```python
import torch

def _softmax(x, axis=-1):
    return torch.softmax(x, dim=axis)

def _gelu(x):
    return torch.nn.functional.gelu(x, approximate='tanh')

def _silu(x):
    return torch.nn.functional.silu(x)

def _sigmoid(x):
    return torch.sigmoid(x)

def _layernorm(x, axis=-1, eps=1e-05):
    m = x.mean(dim=axis, keepdim=True)
    v = x.var(dim=axis, keepdim=True, unbiased=False)
    return (x - m) / torch.sqrt(v + eps)

def _scatter_add(tgt, idx, src, axis=0):
    return tgt.clone().index_add_(axis, idx, src)

def _segment_sum(src, seg, n):
    out = torch.zeros((n,) + src.shape[1:], dtype=src.dtype)
    return out.index_add_(0, seg, src)

def _pad(x, pw):
    flat = tuple((v for pair in reversed(pw) for v in pair))
    return torch.nn.functional.pad(x, flat)
kernel_name = 'adaLN_cond__gqa_block_cached__glu_gated_geglu__decode'

def setup_inputs():

    _w = lambda a: (lambda t: t.to(torch.bfloat16) if t.is_floating_point() else t)(torch.as_tensor(a))

    def _A_setup():
        B, Sq, D = (1, 1, 640)
        Hq, Hkv, Sk, Dh = (32, 32, 4096, 128)
        assert Hq % Hkv == 0, (Hq, Hkv)
        return {'x': _w((lambda *s: torch.randn(*s, dtype=torch.bfloat16) * 0.05)(B, Sq, D)), 'k_cache': _w((lambda *s: torch.randn(*s, dtype=torch.bfloat16) * 3.2)(B, Hkv, Sk, Dh)), 'v_cache': _w((lambda *s: torch.randn(*s, dtype=torch.bfloat16) * 3.2)(B, Hkv, Sk, Dh)), 'wq': _w((lambda *s: torch.randn(*s, dtype=torch.bfloat16) * 3.2)(D, Hq * Dh) * D ** (-0.5)), 'wo': _w((lambda *s: torch.randn(*s, dtype=torch.bfloat16) * 3.2)(Hq * Dh, D) * (Hq * Dh) ** (-0.5))}
    _A_in = _A_setup()

    def _B_setup():
        B, S, Dm, Df = (1, 1, 640, 1728)
        return {'x': _w((lambda *s: torch.randn(*s, dtype=torch.bfloat16) * 0.05)(B, S, Dm)), 'w_gate': _w((lambda *s: torch.randn(*s, dtype=torch.bfloat16) * 3.2)(Dm, Df) * Dm ** (-0.5)), 'w_up': _w((lambda *s: torch.randn(*s, dtype=torch.bfloat16) * 3.2)(Dm, Df) * Dm ** (-0.5)), 'w_down': _w((lambda *s: torch.randn(*s, dtype=torch.bfloat16) * 3.2)(Df, Dm) * Df ** (-0.5))}
    _B_in = _B_setup()
    return {'A_x': _A_in['x'], 'A_k_cache': _A_in['k_cache'], 'A_v_cache': _A_in['v_cache'], 'A_wq': _A_in['wq'], 'A_wo': _A_in['wo'], 'B_x': _B_in['x'], 'B_w_gate': _B_in['w_gate'], 'B_w_up': _B_in['w_up'], 'B_w_down': _B_in['w_down'], 'c': _w((lambda *s: torch.randn(*s, dtype=torch.bfloat16) * 0.1)(1, 640) * 8.0), 'w_mod': _w((lambda *s: torch.randn(*s, dtype=torch.bfloat16) * 0.1)(640, 6 * 640) * 640 ** (-0.5) * 8.0)}

def reference(A_x, A_k_cache, A_v_cache, A_wq, A_wo, B_x, B_w_gate, B_w_up, B_w_down, c, w_mod):

    def _A_ref(x, k_cache, v_cache, wq, wo):
        B, Sq, D = x.shape
        Hq, Dh = (32, 128)
        Hkv = k_cache.shape[1]
        rep = Hq // Hkv
        q = (lambda x, *d: x.permute(*d))((x @ wq).reshape(B, Sq, Hq, Dh), 0, 2, 1, 3)
        k = (lambda x, n, axis: torch.repeat_interleave(x, n, dim=axis))(k_cache, rep, axis=1)
        v = (lambda x, n, axis: torch.repeat_interleave(x, n, dim=axis))(v_cache, rep, axis=1)
        scores = torch.einsum('bhqd,bhkd->bhqk', q, k) * Dh ** (-0.5)
        p = _softmax(scores, axis=-1)
        o = torch.einsum('bhqk,bhkd->bhqd', p, v)
        return (lambda x, *d: x.permute(*d))(o, 0, 2, 1, 3).reshape(B, Sq, Hq * Dh) @ wo

    def _B_ref(x, w_gate, w_up, w_down):
        g = torch.einsum('bsd,df->bsf', x, w_gate)
        u = torch.einsum('bsd,df->bsf', x, w_up)
        h = _gelu(g) * u
        return torch.einsum('bsf,fd->bsd', h, w_down)
    x = A_x
    mod = (_silu(c) @ w_mod).reshape(c.shape[0], 6, 1, -1)
    sa, ca, ga, sb, cb, gb = (mod[:, 0].clone(), mod[:, 1].clone(), mod[:, 2].clone(), mod[:, 3].clone(), mod[:, 4].clone(), mod[:, 5].clone())
    h = _layernorm(x, axis=-1) * (1 + ca) + sa
    x = x + ga * _A_ref(h, A_k_cache, A_v_cache, A_wq, A_wo)
    h = _layernorm(x, axis=-1) * (1 + cb) + sb
    x = x + gb * _B_ref(h, B_w_gate, B_w_up, B_w_down)
    return x
```

```cpp
#include <hip/hip_runtime.h>
#include <hip/hip_bf16.h>

typedef unsigned u32;
typedef unsigned long long u64;
typedef u32 u32x4 __attribute__((ext_vector_type(4)));
typedef u32 u32x2 __attribute__((ext_vector_type(2)));
typedef __bf16 bf16x2 __attribute__((ext_vector_type(2)));
typedef float f32x2 __attribute__((ext_vector_type(2)));
typedef __amdgpu_buffer_rsrc_t rsrc_t;

#define KWIN 4
#ifndef NV0
#define NV0 4
#endif
#define NT_KV 2
#define NT_W 0
#define SPIN_CAP 400000u

#define WS_EPOCH 0
#define WS_MOD   1024
#define WS_X1    (WS_MOD + 1920 * 8)
#define WS_HH    (WS_X1 + 320 * 8)
#define WS_ML    (WS_HH + 896 * 8)
#define WS_Y     (WS_ML + 256 * 64)
#define WS_Q     (WS_Y + 32 * 640 * 8)
#define WS_O     (WS_Q + 32768 * 8)
#define WS_END   (WS_O + 32768 * 8)
#define WS_STAMP (1 << 20)

#define FENCE() asm volatile("" ::: "memory")
#define ST(i)

struct Params {
  const unsigned short *x, *k, *v, *wq, *wo, *wg, *wu, *wd, *c, *wm;
  unsigned short* out;
  char* ws;
};

__device__ __forceinline__ float blo(u32 a) { return __uint_as_float(a << 16); }
__device__ __forceinline__ float bhi(u32 a) { return __uint_as_float(a & 0xffff0000u); }
__device__ __forceinline__ u32 pk(float a, float b) {
  bf16x2 r = {(__bf16)a, (__bf16)b};
  return __builtin_bit_cast(u32, r);
}
__device__ __forceinline__ float rb(float a) { return (float)(__bf16)a; }
__device__ __forceinline__ float dot2(u32 a, u32 b, float c) {
  return __builtin_amdgcn_fdot2_f32_bf16(__builtin_bit_cast(bf16x2, a), __builtin_bit_cast(bf16x2, b), c, false);
}
template <int C> __device__ __forceinline__ float dpp(float a) {
  return __int_as_float(__builtin_amdgcn_update_dpp(0, __float_as_int(a), C, 0xf, 0xf, true));
}
#define DPP_X1 0xB1
#define DPP_X2 0x4E
#define DPP_R4 0x124
#define DPP_R8 0x128
__device__ __forceinline__ float sw32(float a, float b) {
  auto r = __builtin_amdgcn_permlane32_swap(__float_as_uint(a), __float_as_uint(b), false, false);
  return __uint_as_float(r[0]) + __uint_as_float(r[1]);
}
__device__ __forceinline__ float sw16(float a, float b) {
  auto r = __builtin_amdgcn_permlane16_swap(__float_as_uint(a), __float_as_uint(b), false, false);
  return __uint_as_float(r[0]) + __uint_as_float(r[1]);
}
__device__ __forceinline__ float mx32(float a) {
  auto r = __builtin_amdgcn_permlane32_swap(__float_as_uint(a), __float_as_uint(a), false, false);
  return fmaxf(__uint_as_float(r[0]), __uint_as_float(r[1]));
}
__device__ __forceinline__ float mx16(float a) {
  auto r = __builtin_amdgcn_permlane16_swap(__float_as_uint(a), __float_as_uint(a), false, false);
  return fmaxf(__uint_as_float(r[0]), __uint_as_float(r[1]));
}
__device__ __forceinline__ float row_sum(float a) {
  a += dpp<DPP_X1>(a); a += dpp<DPP_X2>(a); a += dpp<DPP_R4>(a); a += dpp<DPP_R8>(a); return a;
}
__device__ __forceinline__ float wave_sum(float a) { a = row_sum(a); a = sw16(a, a); return sw32(a, a); }
__device__ __forceinline__ void reduce8(const float (&a)[8], float& r0, float& r1) {
  float b0 = sw32(a[0], a[4]), b1 = sw32(a[1], a[5]), b2 = sw32(a[2], a[6]), b3 = sw32(a[3], a[7]);
  r0 = sw16(b0, b2); r1 = sw16(b1, b3);
}
__device__ __forceinline__ void mac8(float (&acc)[8], u32x4 w, float a) {
#pragma unroll
  for (int j = 0; j < 4; ++j) { acc[2 * j] += a * blo(w[j]); acc[2 * j + 1] += a * bhi(w[j]); }
}
__device__ __forceinline__ void mac8x2(float (&acc)[8], u32x4 w0, u32x4 w1, u32 pair) {
#pragma unroll
  for (int j = 0; j < 4; ++j) {
    acc[2 * j]     = dot2(__builtin_amdgcn_perm(w1[j], w0[j], 0x05040100u), pair, acc[2 * j]);
    acc[2 * j + 1] = dot2(__builtin_amdgcn_perm(w1[j], w0[j], 0x07060302u), pair, acc[2 * j + 1]);
  }
}
__device__ __forceinline__ rsrc_t mkr(const void* ptr, u32 bytes) {
  return __builtin_amdgcn_make_buffer_rsrc((void*)ptr, 0, bytes, 0x00020000);
}
template <int AUX> __device__ __forceinline__ u32x4 ld16(rsrc_t r, u32 off) {
  return __builtin_amdgcn_raw_buffer_load_b128(r, off, 0, AUX);
}
__device__ __forceinline__ u32x2 ldg8(rsrc_t r, u32 off) { return __builtin_amdgcn_raw_buffer_load_b64(r, off, 0, 16); }
__device__ __forceinline__ void stg16(rsrc_t r, u32 off, float a, float b, u32 e) {
  u32x4 v = {__float_as_uint(a), e, __float_as_uint(b), e};
  __builtin_amdgcn_raw_buffer_store_b128(v, r, off, 0, 16);
}
__device__ __forceinline__ void stg8(rsrc_t r, u32 off, u32 a, u32 e) {
  u32x2 v = {a, e};
  __builtin_amdgcn_raw_buffer_store_b64(v, r, off, 0, 16);
}
template <int N> __device__ __forceinline__ void gather16(rsrc_t r, const u32 (&off)[N], u32 e, u32x4 (&v)[N]) {
#pragma nounroll
  for (u32 spins = 0; spins < SPIN_CAP; ++spins) {
    FENCE();
    bool ok = true;
#pragma unroll
    for (int k = 0; k < N; ++k) v[k] = ld16<16>(r, off[k]);
#pragma unroll
    for (int k = 0; k < N; ++k) ok = ok && v[k][1] == e && v[k][3] == e;
    if (__all(ok)) break;
    __builtin_amdgcn_s_sleep(1);
  }
  FENCE();
}
template <int N> __device__ __forceinline__ void gather8(rsrc_t r, const u32 (&off)[N], u32 e, u32 (&v)[N]) {
#pragma nounroll
  for (u32 spins = 0; spins < SPIN_CAP; ++spins) {
    FENCE();
    bool ok = true;
    u32x2 t[N];
#pragma unroll
    for (int k = 0; k < N; ++k) t[k] = ldg8(r, off[k]);
#pragma unroll
    for (int k = 0; k < N; ++k) { v[k] = t[k][0]; ok = ok && t[k][1] == e; }
    if (__all(ok)) break;
    __builtin_amdgcn_s_sleep(1);
  }
  FENCE();
}
__device__ __forceinline__ void bar() {
  asm volatile("s_waitcnt lgkmcnt(0)\n\ts_barrier" ::: "memory");
}
__device__ __forceinline__ void chain_sync(u32* cnt, int lane) {
  asm volatile("s_waitcnt lgkmcnt(0)" ::: "memory");
  if (lane == 0) __hip_atomic_fetch_add(cnt, 1u, __ATOMIC_RELAXED, __HIP_MEMORY_SCOPE_WORKGROUP);
  while (__hip_atomic_load(cnt, __ATOMIC_RELAXED, __HIP_MEMORY_SCOPE_WORKGROUP) < 4u) __builtin_amdgcn_s_sleep(0);
  FENCE();
}
__device__ __forceinline__ void ln_stats(const float (&v)[10], const float (&wt)[10], float& m, float& sd) {
  float s = 0.f;
#pragma unroll
  for (int i = 0; i < 10; ++i) s += v[i] * wt[i];
  const float mean = wave_sum(s) * (1.f / 640.f);
  float q = 0.f;
#pragma unroll
  for (int i = 0; i < 10; ++i) { const float d = v[i] - mean; q += d * d * wt[i]; }
  const float var = wave_sum(q) * (1.f / 640.f);
  m = rb(mean);
  sd = rb(sqrtf(rb(rb(var) + 1e-5f)));
}
__device__ __forceinline__ float modulate(float x, float m, float sd, float sc, float sh) {
  return rb(rb(rb(rb(x - m) / sd) * rb(1.f + sc)) + sh);
}
__device__ __forceinline__ float gelu_tanh(float g) {
  const float z = 0.7978845608028654f * (g + 0.044715f * g * g * g);
  const float t = 1.f - 2.f / (__expf(2.f * z) + 1.f);
  return 0.5f * g * (1.f + t);
}

__global__ void __launch_bounds__(768, 3) fused_block(Params p) {
  __shared__ __attribute__((aligned(16))) float s_part[8][128];
  __shared__ __attribute__((aligned(16))) float s_red[4][128];
  __shared__ __attribute__((aligned(16))) float s_h[640];
  __shared__ __attribute__((aligned(16))) u32 s_q[64];
  __shared__ __attribute__((aligned(16))) f32x2 s_ml[8];
  __shared__ u32 s_cnt[2];
  const int tid = threadIdx.x, lane = tid & 63, g = lane >> 4, ch = lane & 15;
  const int wv = __builtin_amdgcn_readfirstlane(tid >> 6);
  const int b = blockIdx.x, xcd = b & 7, r = b >> 3;
  const int head = xcd * 4 + (r & 3), sp = r >> 2;

  if (wv >= 4) {
    const int w = wv - 4;
    const size_t base = ((size_t)head * 4096 + sp * 512 + w * 64) * 128;
    const rsrc_t rk = mkr(p.k + base, 16384), rv = mkr(p.v + base, 16384);
    u32x4 K[16], V[16];
    bar();
#pragma unroll
    for (int i = 0; i < 16 + NV0; ++i) {
      if (i >= KWIN) asm volatile("s_waitcnt vmcnt(%0)" :: "n"(KWIN - 1) : "memory");
      if (i < 16) K[i] = ld16<NT_KV>(rk, i * 1024 + lane * 16);
      else V[i - 16] = ld16<NT_KV>(rv, (i - 16) * 1024 + lane * 16);
      FENCE();
    }
    if (w == 0) ST(16);
    bar();
#pragma unroll
    for (int i = NV0; i < 16; ++i) V[i] = ld16<NT_KV>(rv, i * 1024 + lane * 16);
    FENCE();
    const u32x4 q = *(const u32x4*)&s_q[ch * 4];
    float sc[16], m = -INFINITY;
#pragma unroll
    for (int i = 0; i < 16; ++i) {
      float a = dot2(K[i][0], q[0], 0.f);
      a = dot2(K[i][1], q[1], a); a = dot2(K[i][2], q[2], a); a = dot2(K[i][3], q[3], a);
      a = row_sum(a);
      sc[i] = rb(rb(a) * 0.08838834764831845f);
      m = fmaxf(m, sc[i]);
    }
    m = mx32(mx16(m));
    float l = 0.f;
    u32 pp[8];
#pragma unroll
    for (int i = 0; i < 8; ++i) {
      const float e0 = __expf(sc[2 * i] - m), e1 = __expf(sc[2 * i + 1] - m);
      l += e0 + e1;
      pp[i] = pk(e0, e1);
    }
    l = sw16(l, l); l = sw32(l, l);
    if (w == 0) ST(17);
    float acc[8] = {0.f, 0.f, 0.f, 0.f, 0.f, 0.f, 0.f, 0.f};
#pragma unroll
    for (int i = 0; i < 8; ++i) mac8x2(acc, V[2 * i], V[2 * i + 1], pp[i]);
    float o0, o1;
    reduce8(acc, o0, o1);
    *(f32x2*)&s_part[w][ch * 8 + g * 2] = f32x2{o0, o1};
    if (lane == 0) s_ml[w] = f32x2{m, l};
    if (w == 0) ST(18);
    bar();
    return;
  }

  const rsrc_t rw = mkr(p.ws, WS_END);
  if (wv == 0) ST(0);
  const bool mod_own = r < 30;
  const int unit = r < 10 ? xcd * 10 + r : 80 + xcd * 20 + (r - 10);
  u32x4 wa[5], wb[5];
  u32 craw[5], xraw[5], xq, eraw;
  {
    const rsrc_t rm = mkr(p.wm, mod_own ? 640 * 3840 * 2 : 0), rc = mkr(p.c, 1280), rx = mkr(p.x, 1280);
    const rsrc_t rq = mkr(p.wq, 640 * 4096 * 2);
#pragma unroll
    for (int j = 0; j < 5; ++j) wa[j] = ld16<NT_W>(rm, ((tid >> 1) + 128 * j) * 7680 + unit * 32 + (tid & 1) * 16);
#pragma unroll
    for (int j = 0; j < 5; ++j) craw[j] = __builtin_amdgcn_raw_buffer_load_b16(rc, ((tid >> 1) + 128 * j) * 2, 0, 0);
    eraw = __builtin_amdgcn_raw_buffer_load_b32(rw, WS_EPOCH, 0, 16);
#pragma unroll
    for (int j = 0; j < 5; ++j) xraw[j] = __builtin_amdgcn_raw_buffer_load_b32(rx, (lane + 64 * j) * 4, 0, 0);
    xq = __builtin_amdgcn_raw_buffer_load_b32(rx, (sp * 40 + wv * 10 + (lane < 10 ? lane : 0)) * 4, 0, 0);
#pragma unroll
    for (int j = 0; j < 5; ++j)
      wb[j] = ld16<NT_W>(rq, (sp * 80 + wv * 20 + g + 4 * j) * 8192 + head * 256 + ch * 16);
  }
  if (tid == 0) { s_cnt[0] = 0u; s_cnt[1] = 0u; }
  __builtin_amdgcn_sched_barrier(0);
  bar();
  __builtin_amdgcn_sched_barrier(0);
  const u32 epoch = __builtin_amdgcn_readfirstlane(eraw) + 1u;
  float xm, xsd, cs[5];
  {
    float xv[10], one[10];
#pragma unroll
    for (int j = 0; j < 5; ++j) {
      xv[2 * j] = blo(xraw[j]); xv[2 * j + 1] = bhi(xraw[j]); one[2 * j] = 1.f; one[2 * j + 1] = 1.f;
      cs[j] = blo(craw[j]);
    }
    ln_stats(xv, one, xm, xsd);
  }
  if (mod_own) {
    float acc[8] = {0.f, 0.f, 0.f, 0.f, 0.f, 0.f, 0.f, 0.f};
#pragma unroll
    for (int j = 0; j < 5; ++j) mac8(acc, wa[j], rb(cs[j] / (1.f + __expf(-cs[j]))));
    float a0, a1;
    reduce8(acc, a0, a1);
    a0 += dpp<DPP_X2>(a0); a0 += dpp<DPP_R4>(a0); a0 += dpp<DPP_R8>(a0);
    a1 += dpp<DPP_X2>(a1); a1 += dpp<DPP_R4>(a1); a1 += dpp<DPP_R8>(a1);
    if (ch < 2) *(f32x2*)&s_red[wv][ch * 8 + g * 2] = f32x2{a0, a1};
    chain_sync(&s_cnt[0], lane);
    if (wv == 0 && lane < 8) {
      f32x2 t = *(f32x2*)&s_red[0][lane * 2];
#pragma unroll
      for (int k = 1; k < 4; ++k) t += *(f32x2*)&s_red[k][lane * 2];
      stg8(rw, WS_MOD + (unit * 8 + lane) * 8, pk(t[0], t[1]), epoch);
    }
  }
  if (wv == 0) ST(1);
  {
    const int pi = sp * 40 + wv * 10 + (lane < 10 ? lane : 0);
    const u32 off[2] = {(u32)(WS_MOD + pi * 8), (u32)(WS_MOD + (320 + pi) * 8)};
    u32 mv[2];
    gather8<2>(rw, off, epoch, mv);
    if (wv == 0) ST(2);
    const float h0 = modulate(blo(xq), xm, xsd, blo(mv[1]), blo(mv[0]));
    const float h1 = modulate(bhi(xq), xm, xsd, bhi(mv[1]), bhi(mv[0]));
    if (lane < 10) *(f32x2*)&s_h[wv * 20 + lane * 2] = f32x2{h0, h1};
    FENCE();
    float acc[8] = {0.f, 0.f, 0.f, 0.f, 0.f, 0.f, 0.f, 0.f};
#pragma unroll
    for (int j = 0; j < 5; ++j) mac8(acc, wb[j], s_h[wv * 20 + g + 4 * j]);
    float a0, a1;
    reduce8(acc, a0, a1);
    *(f32x2*)&s_part[wv][ch * 8 + g * 2] = f32x2{a0, a1};
    chain_sync(&s_cnt[1], lane);
    if (wv == 0) {
      f32x2 t = *(f32x2*)&s_part[0][lane * 2];
#pragma unroll
      for (int k = 1; k < 4; ++k) t += *(f32x2*)&s_part[k][lane * 2];
      stg16(rw, WS_Q + ((head * 8 + sp) * 64 + lane) * 16, t[0], t[1], epoch);
      ST(3);
      u32 qo[8];
      u32x4 qv[8];
#pragma unroll
      for (int k = 0; k < 8; ++k) qo[k] = WS_Q + ((head * 8 + k) * 64 + lane) * 16;
      gather16<8>(rw, qo, epoch, qv);
      float q0 = 0.f, q1 = 0.f;
#pragma unroll
      for (int k = 0; k < 8; ++k) { q0 += __uint_as_float(qv[k][0]); q1 += __uint_as_float(qv[k][2]); }
      s_q[lane] = pk(q0, q1);
      ST(4);
    }
  }
  bar();
  const bool wo_own = sp < 5, gu_own = r < 27, rd_own = r >= 27, dn_own = r >= 20 && r < 30;
  const int gu = xcd * 27 + r, rd = xcd * 5 + (r - 27), dn = xcd * 10 + (r - 20);
  u32x4 wt[8], wg[5];
  {
    const rsrc_t rt = mkr(wo_own ? p.wo : p.wd, wo_own ? 4096 * 640 * 2 : dn_own ? 1728 * 640 * 2 : 0);
    const u32 t0 = wo_own ? (head * 128 + wv * 32 + g * 2) * 1280 + sp * 256 + ch * 16 : tid * 2560 + dn * 16;
    const u32 ts = wo_own ? 8 * 1280 : 512 * 1280;
#pragma unroll
    for (int j = 0; j < 8; ++j) wt[j] = ld16<NT_W>(rt, t0 + (j >> 1) * ts + (j & 1) * 1280);
  }
  {
    const rsrc_t rg = mkr(wv < 2 ? p.wg : p.wu, gu_own ? 640 * 1728 * 2 : 0);
#pragma unroll
    for (int j = 0; j < 5; ++j) wg[j] = ld16<NT_W>(rg, ((tid & 127) + 128 * j) * 3456 + gu * 16);
  }
  u32x4 sbv[3], cbv[3];
  u32 gx[2], xr = 0;
  if (wv == 0) {
    if (gu_own) {
      u32 o6[6];
      u32x4 t6[6];
#pragma unroll
      for (int k = 0; k < 3; ++k) {
        const int i = k < 2 ? lane + 64 * k : 128 + (lane & 31);
        o6[k] = WS_MOD + 960 * 8 + i * 16; o6[3 + k] = WS_MOD + 1280 * 8 + i * 16;
      }
      gather16<6>(rw, o6, epoch, t6);
#pragma unroll
      for (int k = 0; k < 3; ++k) { sbv[k] = t6[k]; cbv[k] = t6[3 + k]; }
    }
    const u32 o2[2] = {(u32)(WS_MOD + (640 + (rd_own ? rd * 8 : 0) + (lane & 7)) * 8),
                       (u32)(WS_MOD + (1600 + (dn_own ? dn * 4 : 0) + (lane & 3)) * 8)};
    gather8<2>(rw, o2, epoch, gx);
    xr = ((const u32*)p.x)[(rd_own ? rd * 8 : 0) + (lane & 7)];
    ST(5);
  }
  FENCE();
  bar();
  if (wv == 0) {
    ST(6);
    f32x2 ml[8];
    float M = -INFINITY, L = 0.f;
#pragma unroll
    for (int k = 0; k < 8; ++k) { ml[k] = s_ml[k]; M = fmaxf(M, ml[k][0]); }
    f32x2 o = {0.f, 0.f};
#pragma unroll
    for (int k = 0; k < 8; ++k) {
      const float f = __expf(ml[k][0] - M);
      L += f * ml[k][1];
      o += f * *(f32x2*)&s_part[k][lane * 2];
    }
    stg16(rw, WS_O + ((head * 8 + sp) * 64 + lane) * 16, o[0], o[1], epoch);
    if (lane == 0) stg16(rw, WS_ML + (head * 8 + sp) * 64, M, L, epoch);
    ST(7);
  }
  if (wo_own) {
    u32 oo[4];
    u32x4 ov[4];
#pragma unroll
    for (int k = 0; k < 2; ++k) {
      oo[k] = WS_O + ((head * 8 + g + 4 * k) * 64 + wv * 16 + ch) * 16;
      oo[2 + k] = WS_ML + (head * 8 + g + 4 * k) * 64;
    }
    gather16<4>(rw, oo, epoch, ov);
    if (wv == 0) ST(8);
    const float m0 = __uint_as_float(ov[2][0]), m1 = __uint_as_float(ov[3][0]);
    const float M = mx32(mx16(fmaxf(m0, m1)));
    const float f0 = __expf(m0 - M), f1 = __expf(m1 - M);
    float L = f0 * __uint_as_float(ov[2][2]) + f1 * __uint_as_float(ov[3][2]);
    float o0 = f0 * __uint_as_float(ov[0][0]) + f1 * __uint_as_float(ov[1][0]);
    float o1 = f0 * __uint_as_float(ov[0][2]) + f1 * __uint_as_float(ov[1][2]);
    L = sw16(L, L); L = sw32(L, L);
    o0 = sw16(o0, o0); o0 = sw32(o0, o0);
    o1 = sw16(o1, o1); o1 = sw32(o1, o1);
    const float il = 1.f / L;
    const u32 op = pk(o0 * il, o1 * il);
    float acc[8] = {0.f, 0.f, 0.f, 0.f, 0.f, 0.f, 0.f, 0.f};
#pragma unroll
    for (int j = 0; j < 4; ++j)
      mac8x2(acc, wt[2 * j], wt[2 * j + 1], (u32)__builtin_amdgcn_ds_bpermute((4 * j + g) * 4, (int)op));
    float a0, a1;
    reduce8(acc, a0, a1);
    *(f32x2*)&s_red[wv][ch * 8 + g * 2] = f32x2{a0, a1};
    bar();
    if (wv == 0) {
      f32x2 t = *(f32x2*)&s_red[0][lane * 2];
#pragma unroll
      for (int k = 1; k < 4; ++k) t += *(f32x2*)&s_red[k][lane * 2];
      stg16(rw, WS_Y + (head * 320 + sp * 64 + lane) * 16, t[0], t[1], epoch);
      ST(9);
    }
  }
  if (rd_own && wv == 0) {
    u32 yo[4];
    u32x4 yv[4];
#pragma unroll
    for (int k = 0; k < 4; ++k) yo[k] = WS_Y + (((lane >> 3) + 8 * k) * 320 + rd * 8 + (lane & 7)) * 16;
    gather16<4>(rw, yo, epoch, yv);
    ST(10);
    float y0 = 0.f, y1 = 0.f;
#pragma unroll
    for (int k = 0; k < 4; ++k) { y0 += __uint_as_float(yv[k][0]); y1 += __uint_as_float(yv[k][2]); }
    y0 += dpp<DPP_R8>(y0); y0 = sw16(y0, y0); y0 = sw32(y0, y0);
    y1 += dpp<DPP_R8>(y1); y1 = sw16(y1, y1); y1 = sw32(y1, y1);
    const u32 x1 = pk(blo(xr) + rb(blo(gx[0]) * rb(y0)), bhi(xr) + rb(bhi(gx[0]) * rb(y1)));
    if (lane < 8) stg8(rw, WS_X1 + (rd * 8 + lane) * 8, x1, epoch);
    ST(11);
  }
  if (gu_own) {
    if (wv == 0) {
      u32 xo[3];
      u32x4 xv[3];
#pragma unroll
      for (int k = 0; k < 3; ++k) xo[k] = WS_X1 + (k < 2 ? lane + 64 * k : 128 + (lane & 31)) * 16;
      gather16<3>(rw, xo, epoch, xv);
      ST(12);
      float v[12], a[10], one[10];
#pragma unroll
      for (int k = 0; k < 3; ++k) {
        v[4 * k] = blo(xv[k][0]); v[4 * k + 1] = bhi(xv[k][0]); v[4 * k + 2] = blo(xv[k][2]); v[4 * k + 3] = bhi(xv[k][2]);
      }
#pragma unroll
      for (int i = 0; i < 8; ++i) { a[i] = v[i]; one[i] = 1.f; }
      a[8] = lane < 32 ? v[8] : v[10]; a[9] = lane < 32 ? v[9] : v[11]; one[8] = 1.f; one[9] = 1.f;
      float m, sd;
      ln_stats(a, one, m, sd);
#pragma unroll
      for (int k = 0; k < 3; ++k) {
        const float h0 = modulate(v[4 * k], m, sd, blo(cbv[k][0]), blo(sbv[k][0]));
        const float h1 = modulate(v[4 * k + 1], m, sd, bhi(cbv[k][0]), bhi(sbv[k][0]));
        const float h2 = modulate(v[4 * k + 2], m, sd, blo(cbv[k][2]), blo(sbv[k][2]));
        const float h3 = modulate(v[4 * k + 3], m, sd, bhi(cbv[k][2]), bhi(sbv[k][2]));
        if (k < 2 || lane < 32) *(float4*)&s_h[(lane + 64 * k) * 4] = make_float4(h0, h1, h2, h3);
      }
    }
    bar();
    float acc[8] = {0.f, 0.f, 0.f, 0.f, 0.f, 0.f, 0.f, 0.f};
#pragma unroll
    for (int j = 0; j < 5; ++j) mac8(acc, wg[j], s_h[(tid & 127) + 128 * j]);
    float a0, a1;
    reduce8(acc, a0, a1);
    a0 = row_sum(a0); a1 = row_sum(a1);
    if (ch == 0) *(f32x2*)&s_part[wv][g * 2] = f32x2{a0, a1};
    bar();
    if (wv == 0 && lane < 4) {
      const f32x2 gg = *(f32x2*)&s_part[0][lane * 2] + *(f32x2*)&s_part[1][lane * 2];
      const f32x2 uu = *(f32x2*)&s_part[2][lane * 2] + *(f32x2*)&s_part[3][lane * 2];
      stg8(rw, WS_HH + (gu * 4 + lane) * 8,
           pk(rb(gelu_tanh(rb(gg[0]))) * rb(uu[0]), rb(gelu_tanh(rb(gg[1]))) * rb(uu[1])), epoch);
    }
    if (wv == 0) ST(13);
  }
  if (dn_own) {
    u32 ho[5], hv[5];
#pragma unroll
    for (int k = 0; k < 4; ++k) ho[k] = WS_HH + (k < 3 || tid < 96 ? tid + 256 * k : tid) * 8;
    ho[4] = WS_X1 + (dn * 4 + (lane & 3)) * 8;
    gather8<5>(rw, ho, epoch, hv);
    if (wv == 0) ST(14);
    float acc[8] = {0.f, 0.f, 0.f, 0.f, 0.f, 0.f, 0.f, 0.f};
    if (tid >= 96) hv[3] = 0u;
#pragma unroll
    for (int j = 0; j < 4; ++j) mac8x2(acc, wt[2 * j], wt[2 * j + 1], hv[j]);
    float a0, a1;
    reduce8(acc, a0, a1);
    a0 = row_sum(a0); a1 = row_sum(a1);
    if (ch == 0) *(f32x2*)&s_red[wv][g * 2] = f32x2{a0, a1};
    bar();
    if (wv == 0 && lane < 4) {
      f32x2 t = *(f32x2*)&s_red[0][lane * 2];
#pragma unroll
      for (int k = 1; k < 4; ++k) t += *(f32x2*)&s_red[k][lane * 2];
      ((u32*)p.out)[dn * 4 + lane] = pk(blo(hv[4]) + rb(blo(gx[1]) * rb(t[0])), bhi(hv[4]) + rb(bhi(gx[1]) * rb(t[1])));
      if (dn == 0 && lane == 0) __builtin_amdgcn_raw_buffer_store_b32(epoch, rw, WS_EPOCH, 0, 16);
    }
    if (wv == 0) ST(15);
  }
}

extern "C" void kernel_launch(void* const* d_in, const int* in_sizes, int n_in, void* d_out, int out_size,
                              void* d_ws, size_t ws_size, hipStream_t stream) {
  Params p;
  p.x = (const unsigned short*)d_in[0];  p.k = (const unsigned short*)d_in[1];
  p.v = (const unsigned short*)d_in[2];  p.wq = (const unsigned short*)d_in[3];
  p.wo = (const unsigned short*)d_in[4]; p.wg = (const unsigned short*)d_in[6];
  p.wu = (const unsigned short*)d_in[7]; p.wd = (const unsigned short*)d_in[8];
  p.c = (const unsigned short*)d_in[9];  p.wm = (const unsigned short*)d_in[10];
  p.out = (unsigned short*)d_out;        p.ws = (char*)d_ws;
  fused_block<<<256, 768, 0, stream>>>(p);
}
```

```cpp
#include <hip/hip_runtime.h>
#include <hip/hip_bf16.h>

typedef unsigned u32;
typedef unsigned long long u64;
typedef u32 u32x4 __attribute__((ext_vector_type(4)));
typedef u32 u32x2 __attribute__((ext_vector_type(2)));
typedef __bf16 bf16x2 __attribute__((ext_vector_type(2)));
typedef float f32x2 __attribute__((ext_vector_type(2)));
typedef __amdgpu_buffer_rsrc_t rsrc_t;

#define KWIN 4
#ifndef NV0
#define NV0 4
#endif
#ifndef NV1
#define NV1 16
#endif
#define NT_KV 2
#define NT_W 2
#define SPIN_CAP 400000u

#define WS_EPOCH 0
#define WS_ACK   256
#define WS_X1    1024
#define WS_ML    8192
#define WS_MOD   (WS_ML + 256 * 64)
#define WS_Y     (WS_MOD + 4 * 3840 * 8)
#define WS_D     (WS_Y + 16 * 640 * 8)
#define WS_Q     (WS_D + 27 * 640 * 8)
#define WS_O     (WS_Q + 32 * 5 * 128 * 8)
#define WS_END   (WS_O + 32 * 7 * 128 * 8)
#define WS_STAMP (1 << 20)

#define FENCE() asm volatile("" ::: "memory")
#define ST(i)

struct Params {
  const unsigned short *x, *k, *v, *wq, *wo, *wg, *wu, *wd, *c, *wm;
  unsigned short* out;
  char* ws;
};

__device__ __forceinline__ float blo(u32 a) { return __uint_as_float(a << 16); }
__device__ __forceinline__ float bhi(u32 a) { return __uint_as_float(a & 0xffff0000u); }
__device__ __forceinline__ u32 pk(float a, float b) {
  bf16x2 r = {(__bf16)a, (__bf16)b};
  return __builtin_bit_cast(u32, r);
}
__device__ __forceinline__ float rb(float a) { return (float)(__bf16)a; }
__device__ __forceinline__ float dot2(u32 a, u32 b, float c) {
  return __builtin_amdgcn_fdot2_f32_bf16(__builtin_bit_cast(bf16x2, a), __builtin_bit_cast(bf16x2, b), c, false);
}
template <int C> __device__ __forceinline__ float dpp(float a) {
  return __int_as_float(__builtin_amdgcn_update_dpp(0, __float_as_int(a), C, 0xf, 0xf, true));
}
#define DPP_X1 0xB1
#define DPP_X2 0x4E
#define DPP_R4 0x124
#define DPP_R8 0x128
__device__ __forceinline__ float sw32(float a, float b) {
  auto r = __builtin_amdgcn_permlane32_swap(__float_as_uint(a), __float_as_uint(b), false, false);
  return __uint_as_float(r[0]) + __uint_as_float(r[1]);
}
__device__ __forceinline__ float sw16(float a, float b) {
  auto r = __builtin_amdgcn_permlane16_swap(__float_as_uint(a), __float_as_uint(b), false, false);
  return __uint_as_float(r[0]) + __uint_as_float(r[1]);
}
__device__ __forceinline__ float mx32(float a) {
  auto r = __builtin_amdgcn_permlane32_swap(__float_as_uint(a), __float_as_uint(a), false, false);
  return fmaxf(__uint_as_float(r[0]), __uint_as_float(r[1]));
}
__device__ __forceinline__ float mx16(float a) {
  auto r = __builtin_amdgcn_permlane16_swap(__float_as_uint(a), __float_as_uint(a), false, false);
  return fmaxf(__uint_as_float(r[0]), __uint_as_float(r[1]));
}
__device__ __forceinline__ float row_sum(float a) {
  a += dpp<DPP_X1>(a); a += dpp<DPP_X2>(a); a += dpp<DPP_R4>(a); a += dpp<DPP_R8>(a); return a;
}
__device__ __forceinline__ float wave_sum(float a) { a = row_sum(a); a = sw16(a, a); return sw32(a, a); }
__device__ __forceinline__ void reduce8(const float (&a)[8], float& r0, float& r1) {
  float b0 = sw32(a[0], a[4]), b1 = sw32(a[1], a[5]), b2 = sw32(a[2], a[6]), b3 = sw32(a[3], a[7]);
  r0 = sw16(b0, b2); r1 = sw16(b1, b3);
}
__device__ __forceinline__ void mac8(float (&acc)[8], u32x4 w, float a) {
#pragma unroll
  for (int j = 0; j < 4; ++j) { acc[2 * j] += a * blo(w[j]); acc[2 * j + 1] += a * bhi(w[j]); }
}
__device__ __forceinline__ void mac8x2(float (&acc)[8], u32x4 w0, u32x4 w1, u32 pair) {
#pragma unroll
  for (int j = 0; j < 4; ++j) {
    acc[2 * j]     = dot2(__builtin_amdgcn_perm(w1[j], w0[j], 0x05040100u), pair, acc[2 * j]);
    acc[2 * j + 1] = dot2(__builtin_amdgcn_perm(w1[j], w0[j], 0x07060302u), pair, acc[2 * j + 1]);
  }
}
__device__ __forceinline__ rsrc_t mkr(const void* ptr, u32 bytes) {
  return __builtin_amdgcn_make_buffer_rsrc((void*)ptr, 0, bytes, 0x00020000);
}
template <int AUX> __device__ __forceinline__ u32x4 ld16(rsrc_t r, u32 off, u32 soff = 0) {
  return __builtin_amdgcn_raw_buffer_load_b128(r, off, soff, AUX);
}
__device__ __forceinline__ u32x2 ldg8(rsrc_t r, u32 off) { return __builtin_amdgcn_raw_buffer_load_b64(r, off, 0, 16); }
__device__ __forceinline__ void stg16(rsrc_t r, u32 off, float a, float b, u32 e) {
  u32x4 v = {__float_as_uint(a), e, __float_as_uint(b), e};
  __builtin_amdgcn_raw_buffer_store_b128(v, r, off, 0, 16);
}
__device__ __forceinline__ void stg8(rsrc_t r, u32 off, u32 a, u32 e) {
  u32x2 v = {a, e};
  __builtin_amdgcn_raw_buffer_store_b64(v, r, off, 0, 16);
}
template <int N, int S> __device__ __forceinline__ void gather16(rsrc_t r, u32 off, u32 offl, u32 e, u32x4 (&v)[N]) {
#pragma nounroll
  for (u32 spins = 0; spins < SPIN_CAP; ++spins) {
    FENCE();
    u32 bad = 0;
#pragma unroll
    for (int k = 0; k < N; ++k) v[k] = k < N - 1 ? ld16<16>(r, off, k * S) : ld16<16>(r, offl, k * S);
#pragma unroll
    for (int k = 0; k < N; ++k) bad |= (v[k][1] ^ e) | (v[k][3] ^ e);
    if (__all(bad == 0)) break;
    __builtin_amdgcn_s_sleep(1);
  }
  FENCE();
}
__device__ __forceinline__ u32 gather8(rsrc_t r, u32 off, u32 e) {
  u32x2 t;
#pragma nounroll
  for (u32 spins = 0; spins < SPIN_CAP; ++spins) {
    FENCE();
    t = ldg8(r, off);
    if (__all(t[1] == e)) break;
    __builtin_amdgcn_s_sleep(1);
  }
  FENCE();
  return t[0];
}
template <int N, int S> __device__ __forceinline__ f32x2 gather_sum(rsrc_t r, u32 off, u32 e) {
  u32x4 v[N];
  gather16<N, S>(r, off, off, e, v);
  f32x2 t = {__uint_as_float(v[0][0]), __uint_as_float(v[0][2])};
#pragma unroll
  for (int k = 1; k < N; ++k) t += f32x2{__uint_as_float(v[k][0]), __uint_as_float(v[k][2])};
  return t;
}
__device__ __forceinline__ void bar() {
  asm volatile("s_waitcnt lgkmcnt(0)\n\ts_barrier" ::: "memory");
}
__device__ __forceinline__ void lds_arrive(u32* cnt, int lane) {
  asm volatile("s_waitcnt lgkmcnt(0)" ::: "memory");
  if (lane == 0) __hip_atomic_fetch_add(cnt, 1u, __ATOMIC_RELAXED, __HIP_MEMORY_SCOPE_WORKGROUP);
}
__device__ __forceinline__ void lds_wait(u32* cnt, u32 n) {
  while (__hip_atomic_load(cnt, __ATOMIC_RELAXED, __HIP_MEMORY_SCOPE_WORKGROUP) < n) __builtin_amdgcn_s_sleep(1);
  FENCE();
}
__device__ __forceinline__ void ln_stats(const u32 (&x)[5], float& m, float& isd) {
  float s = 0.f;
#pragma unroll
  for (int i = 0; i < 5; ++i) s += blo(x[i]) + bhi(x[i]);
  const float mean = wave_sum(s) * (1.f / 640.f);
  float q = 0.f;
#pragma unroll
  for (int i = 0; i < 5; ++i) { const float d0 = blo(x[i]) - mean, d1 = bhi(x[i]) - mean; q += d0 * d0 + d1 * d1; }
  const float var = wave_sum(q) * (1.f / 640.f);
  m = rb(mean);
  isd = __builtin_amdgcn_rcpf(rb(__builtin_amdgcn_sqrtf(rb(rb(var) + 1e-5f))));
}
__device__ __forceinline__ u32 modulate(u32 x, float m, float isd, u32 sc, u32 sh) {
  return pk(rb(rb(rb(blo(x) - m) * isd) * rb(1.f + blo(sc))) + blo(sh),
            rb(rb(rb(bhi(x) - m) * isd) * rb(1.f + bhi(sc))) + bhi(sh));
}
__device__ __forceinline__ u32 resid(u32 x, u32 gate, float y0, float y1) {
  return pk(blo(x) + rb(blo(gate) * rb(y0)), bhi(x) + rb(bhi(gate) * rb(y1)));
}
__device__ __forceinline__ void preperm(u32x4 w0, u32x4 w1, u32 (&o)[8]) {
#pragma unroll
  for (int j = 0; j < 4; ++j) {
    o[2 * j] = __builtin_amdgcn_perm(w1[j], w0[j], 0x05040100u);
    o[2 * j + 1] = __builtin_amdgcn_perm(w1[j], w0[j], 0x07060302u);
  }
  asm volatile("" : "+v"(o[0]), "+v"(o[1]), "+v"(o[2]), "+v"(o[3]), "+v"(o[4]), "+v"(o[5]), "+v"(o[6]), "+v"(o[7]));
  __builtin_amdgcn_sched_barrier(0);
}
__device__ __forceinline__ void macp(float (&acc)[8], const u32 (&w)[8], u32 pair) {
#pragma unroll
  for (int j = 0; j < 8; ++j) acc[j] = dot2(w[j], pair, acc[j]);
}
#define ZERO8 {0.f, 0.f, 0.f, 0.f, 0.f, 0.f, 0.f, 0.f}
__device__ __forceinline__ float gelu_tanh(float g) {
  const float z = 0.7978845608028654f * (g + 0.044715f * g * g * g);
  const float t = 1.f - 2.f * __builtin_amdgcn_rcpf(__expf(2.f * z) + 1.f);
  return 0.5f * g * (1.f + t);
}

__device__ __forceinline__ void mod_issue(const Params& p, bool own, int unit, int tid, u32x4 (&wa)[10], u32 (&craw)[10]) {
  const rsrc_t rm = mkr(p.wm, own ? 640 * 3840 * 2 : 0), rc = mkr(p.c, own ? 1280 : 0);
  const int row0 = (unit & 3) * 160 + (tid >> 3);
#pragma unroll
  for (int j = 0; j < 10; ++j) wa[j] = ld16<NT_W>(rm, row0 * 7680 + (unit >> 2) * 128 + (tid & 7) * 16, j * 16 * 7680);
#pragma unroll
  for (int j = 0; j < 10; ++j) craw[j] = __builtin_amdgcn_raw_buffer_load_b16(rc, row0 * 2, j * 32, 0);
}
__device__ __forceinline__ void mod_finish(rsrc_t rw, int unit, int wv, int lane, u32 epoch, const u32x4 (&wa)[10],
                                           const u32 (&craw)[10], float* s_t, u32* cnt) {
  float acc[8] = ZERO8;
#pragma unroll
  for (int j = 0; j < 10; ++j) {
    const float cv = blo(craw[j]);
    mac8(acc, wa[j], rb(cv * __builtin_amdgcn_rcpf(1.f + __expf(-cv))));
  }
  float a0, a1;
  reduce8(acc, a0, a1);
  a0 += dpp<DPP_R8>(a0); a1 += dpp<DPP_R8>(a1);
  if (wv == 1) { *(f32x2*)&s_t[lane * 2] = f32x2{a0, a1}; lds_arrive(cnt, lane); }
  else {
    lds_wait(cnt, 1);
    const f32x2 t = *(f32x2*)&s_t[lane * 2];
    if (!(lane & 8))
      stg16(rw, WS_MOD + ((unit & 3) * 3840 + (unit >> 2) * 64 + (lane & 7) * 8 + (lane >> 4) * 2) * 8, a0 + t[0], a1 + t[1], epoch);
  }
}

__global__ void __launch_bounds__(768, 3) fused_block(Params p) {
  __shared__ __attribute__((aligned(16))) float s_big[8][640];
  __shared__ __attribute__((aligned(16))) float s_part[10][128];
  __shared__ __attribute__((aligned(16))) float s_y[2][640];
  __shared__ __attribute__((aligned(16))) u32 s_w[10][64];
  __shared__ __attribute__((aligned(16))) u32 s_q[64];
  __shared__ __attribute__((aligned(16))) f32x2 s_ml[10];
  __shared__ u32 s_cnt[4];
  const int tid = threadIdx.x, lane = tid & 63, g = lane >> 4, ch = lane & 15, c8 = lane & 7, r8 = lane >> 3;
  const int wv = __builtin_amdgcn_readfirstlane(tid >> 6);
  const int b = blockIdx.x, xcd = b & 7, r = b >> 3;
  const bool attn = r < 28;
  const int head = xcd * 4 + (r & 3), sp = r >> 2;
  const int ex = (r - 28) * 8 + xcd;
  const int nsw = sp == 6 ? 10 : 9;
  if (wv >= (attn ? 2 + nsw : ex < 27 ? 10 : 8)) return;

  if (attn && wv >= 2) {
    const int w = wv - 2;
    const size_t base = ((size_t)head * 4096 + (sp * 9 + w) * 64) * 128;
    const rsrc_t rk = mkr(p.k + base, 16384), rv = mkr(p.v + base, 16384);
    u32x4 K[16], V[16];
    bar();
#pragma unroll
    for (int i = 0; i < 16 + NV0; ++i) {
      if (i >= KWIN) asm volatile("s_waitcnt vmcnt(%0)" :: "n"(KWIN - 1) : "memory");
      if (i < 16) K[i] = ld16<NT_KV>(rk, lane * 16, i * 1024);
      else V[i - 16] = ld16<NT_KV>(rv, lane * 16, (i - 16) * 1024);
      FENCE();
    }
    if (w == 0) ST(16);
    bar();
#pragma unroll
    for (int i = NV0; i < NV1; ++i) V[i] = ld16<NT_KV>(rv, lane * 16, i * 1024);
    FENCE();
    const u32x4 q = *(const u32x4*)&s_q[ch * 4];
    float sc[16], m = -INFINITY;
#pragma unroll
    for (int i = 0; i < 16; ++i) {
      float a = dot2(K[i][0], q[0], 0.f);
      a = dot2(K[i][1], q[1], a); a = dot2(K[i][2], q[2], a); a = dot2(K[i][3], q[3], a);
      a = row_sum(a);
      sc[i] = rb(rb(a) * 0.08838834764831845f);
      m = fmaxf(m, sc[i]);
    }
    m = mx32(mx16(m));
    float l = 0.f;
    u32 pp[8];
#pragma unroll
    for (int i = 0; i < 8; ++i) {
      const float e0 = __expf(sc[2 * i] - m), e1 = __expf(sc[2 * i + 1] - m);
      l += e0 + e1;
      pp[i] = pk(e0, e1);
    }
    l = sw16(l, l); l = sw32(l, l);
    if (lane == 0) s_ml[w] = f32x2{m, l};
    lds_arrive(&s_cnt[2], lane);
    if (w == 0) ST(17);
#pragma unroll
    for (int i = NV1; i < 16; ++i) V[i] = ld16<NT_KV>(rv, lane * 16, i * 1024);
    FENCE();
    float acc[8] = ZERO8;
#pragma unroll
    for (int i = 0; i < 8; ++i) mac8x2(acc, V[2 * i], V[2 * i + 1], pp[i]);
    float o0, o1;
    reduce8(acc, o0, o1);
    *(f32x2*)&s_part[w][ch * 8 + g * 2] = f32x2{o0, o1};
    if (w == 0) ST(18);
    bar();
    return;
  }

  const rsrc_t rw = mkr(p.ws, WS_END), rx = mkr(p.x, 1280);
  if (wv == 0) ST(0);
  const bool qo_own = attn && sp < 5;
#define PROLOGUE_A(MOD_OWN, UNIT)                                                                                     \
  const bool mod_own = (MOD_OWN);                                                                                     \
  const int unit = (UNIT);                                                                                            \
  u32x4 wa[10], wb[16];                                                                                               \
  u32 craw[10], xraw[5];                                                                                              \
  mod_issue(p, mod_own, unit, tid, wa, craw);                                                                         \
  const u32 eraw = __builtin_amdgcn_raw_buffer_load_b32(rw, WS_EPOCH, 0, 16);                                         \
  _Pragma("unroll") for (int j = 0; j < 5; ++j) xraw[j] = __builtin_amdgcn_raw_buffer_load_b32(rx, lane * 4, j * 256, 0);
#define PROLOGUE_B()                                                                                                  \
  if (tid == 0) { s_cnt[0] = 0u; s_cnt[1] = 0u; s_cnt[2] = 0u; }                                                      \
  __builtin_amdgcn_sched_barrier(0);                                                                                  \
  bar();                                                                                                       \
  __builtin_amdgcn_sched_barrier(0);                                                                                  \
  const u32 epoch = __builtin_amdgcn_readfirstlane(eraw) + 1u;                                                        \
  if (mod_own) mod_finish(rw, unit, wv, lane, epoch, wa, craw, &s_part[0][0], &s_cnt[0]);                             \
  if (wv == 0) ST(1);

  if (attn) {
    PROLOGUE_A(true, sp >= 5 ? head * 2 + sp - 5 : sp == 4 ? (head < 16 ? 64 : 192) + head : 80 + head * 4 + sp)
    const u32 xq = __builtin_amdgcn_raw_buffer_load_b32(rx, ((qo_own ? sp * 64 + wv * 32 : 0) + (lane & 31)) * 4, 0, 0);
    {
      const rsrc_t rq = mkr(p.wq, qo_own ? 640 * 4096 * 2 : 0);
#pragma unroll
      for (int j = 0; j < 16; ++j) wb[j] = ld16<NT_W>(rq, (sp * 128 + wv * 64 + g * 16) * 8192 + head * 256 + ch * 16, j * 8192);
    }
    PROLOGUE_B()
    if (qo_own) {
      float xm, xi;
      ln_stats(xraw, xm, xi);
      const f32x2 t = gather_sum<4, 1920 * 16>(rw, WS_MOD + ((lane >> 5) * 320 + sp * 64 + wv * 32 + (lane & 31)) * 16, epoch);
      if (wv == 0) ST(2);
      const u32 mp = pk(t[0], t[1]);
      const auto sc2 = __builtin_amdgcn_permlane32_swap(mp, mp, false, false);
      s_w[wv][lane] = modulate(xq, xm, xi, sc2[1], sc2[0]);
      FENCE();
      const u32x4 h0 = *(const u32x4*)&s_w[wv][g * 8], h1 = *(const u32x4*)&s_w[wv][g * 8 + 4];
      float acc[8] = ZERO8;
#pragma unroll
      for (int j = 0; j < 8; ++j) mac8x2(acc, wb[2 * j], wb[2 * j + 1], j < 4 ? h0[j & 3] : h1[j & 3]);
      float a0, a1;
      reduce8(acc, a0, a1);
      if (wv == 1) { *(f32x2*)&s_y[1][lane * 2] = f32x2{a0, a1}; lds_arrive(&s_cnt[1], lane); }
      else {
        lds_wait(&s_cnt[1], 1);
        const f32x2 u = *(f32x2*)&s_y[1][lane * 2];
        stg16(rw, WS_Q + ((head * 5 + sp) * 64 + ch * 4 + g) * 16, a0 + u[0], a1 + u[1], epoch);
        ST(3);
      }
    }
    if (wv == 0) {
      const f32x2 t = gather_sum<5, 1024>(rw, WS_Q + (head * 5 * 64 + lane) * 16, epoch);
      s_q[lane] = pk(t[0], t[1]);
      ST(4);
    }
    bar();
    const int hp = head >> 1, tile = (head & 1) * 5 + sp;
    {
      const rsrc_t ro = mkr(p.wo, qo_own ? 4096 * 640 * 2 : 0);
#pragma unroll
      for (int j = 0; j < 16; ++j) wb[j] = ld16<NT_W>(ro, ((hp * 2 + wv) * 128 + r8 * 16) * 1280 + tile * 128 + c8 * 16, j * 1280);
    }
    float fw[10], M, L;
    if (wv == 0) {
      lds_wait(&s_cnt[2], nsw);
      const f32x2 ml = ch < nsw ? s_ml[ch] : f32x2{-INFINITY, 0.f};
      M = ml[0];
      M = fmaxf(M, dpp<DPP_X1>(M)); M = fmaxf(M, dpp<DPP_X2>(M)); M = fmaxf(M, dpp<DPP_R4>(M)); M = fmaxf(M, dpp<DPP_R8>(M));
      const float f = __expf(ml[0] - M);
      L = row_sum(f * ml[1]);
      if (lane == 0) stg16(rw, WS_ML + (head * 8 + sp) * 64, M, L, epoch);
#pragma unroll
      for (int k = 0; k < 10; ++k) fw[k] = __int_as_float(__builtin_amdgcn_readlane(__float_as_int(f), k));
      ST(5);
    }
    u32 pb[8][8];
#pragma unroll
    for (int j = 0; j < 8; ++j) preperm(wb[2 * j], wb[2 * j + 1], pb[j]);
    bar();
    if (wv == 0) {
      ST(6);
      f32x2 o = {0.f, 0.f};
#pragma unroll
      for (int k = 0; k < 9; ++k) o += fw[k] * *(f32x2*)&s_part[k][lane * 2];
      if (nsw == 10) o += fw[9] * *(f32x2*)&s_part[9][lane * 2];
      stg16(rw, WS_O + ((head * 7 + sp) * 64 + lane) * 16, o[0], o[1], epoch);
      ST(7);
    }
    if (qo_own) {
      u32x4 ov[8];
      gather16<8, 1024>(rw, WS_O + ((hp * 2 + wv) * 7 * 64 + lane) * 16,
                        WS_ML + ((hp * 2 + wv) * 8 + (c8 < 7 ? c8 : 0)) * 64 - 7 * 1024, epoch, ov);
      if (wv == 0) ST(8);
      const float ms = __uint_as_float(ov[7][0]);
      float Mh = ms;
      Mh = fmaxf(Mh, dpp<DPP_X1>(Mh)); Mh = fmaxf(Mh, dpp<DPP_X2>(Mh)); Mh = fmaxf(Mh, dpp<DPP_R4>(Mh));
      const float es = __expf(ms - Mh);
      float Lh = c8 < 7 ? es * __uint_as_float(ov[7][2]) : 0.f;
      Lh += dpp<DPP_X1>(Lh); Lh += dpp<DPP_X2>(Lh); Lh += dpp<DPP_R4>(Lh);
      const float f = es * __builtin_amdgcn_rcpf(Lh);
      f32x2 t = {0.f, 0.f};
#pragma unroll
      for (int k = 0; k < 7; ++k)
        t += __int_as_float(__builtin_amdgcn_readlane(__float_as_int(f), k)) * f32x2{__uint_as_float(ov[k][0]), __uint_as_float(ov[k][2])};
      s_w[wv][lane] = pk(t[0], t[1]);
      FENCE();
      const u32x4 h0 = *(const u32x4*)&s_w[wv][r8 * 8], h1 = *(const u32x4*)&s_w[wv][r8 * 8 + 4];
      float acc[8] = ZERO8;
#pragma unroll
      for (int j = 0; j < 8; ++j) macp(acc, pb[j], j < 4 ? h0[j & 3] : h1[j & 3]);
      float a0, a1;
      reduce8(acc, a0, a1);
      a0 += dpp<DPP_R8>(a0); a1 += dpp<DPP_R8>(a1);
      if (wv == 1) *(f32x2*)&s_y[0][lane * 2] = f32x2{a0, a1};
      bar();
      if (wv == 0) {
        const f32x2 u = *(f32x2*)&s_y[0][lane * 2];
        if (ch < 8) stg16(rw, WS_Y + (hp * 640 + tile * 64 + c8 * 8 + g * 2) * 8, a0 + u[0], a1 + u[1], epoch);
        ST(9);
      }
    }
  } else if (ex < 27) {
    PROLOGUE_A(wv < 2 && ex < 16, 224 + ex)
    {
      const rsrc_t r0 = mkr(p.wg, 640 * 1728 * 2), r1 = mkr(p.wu, 640 * 1728 * 2);
#pragma unroll
      for (int j = 0; j < 16; ++j) wb[j] = ld16<NT_W>(j < 8 ? r0 : r1, (wv * 64 + r8 * 8) * 3456 + ex * 128 + c8 * 16, (j & 7) * 3456);
    }
    PROLOGUE_B()
    const int cp = tid < 320 ? tid : tid - 320, hf = tid < 320 ? 0 : 1;
    const int rg = tid / 80, c80 = tid - rg * 80;
    u32x4 wd[8];
    {
      const rsrc_t rd = mkr(p.wd, 1728 * 640 * 2);
#pragma unroll
      for (int j = 0; j < 8; ++j) wd[j] = ld16<NT_W>(rd, (ex * 64 + rg * 8) * 1280 + c80 * 16, j * 1280);
    }
    u32* s_m = (u32*)&s_big[0][0];
    {
      const f32x2 t = gather_sum<4, 1920 * 16>(rw, WS_MOD + (640 + tid) * 16, epoch);
      const f32x2 u = gather_sum<4, 1920 * 16>(rw, WS_MOD + (1280 + cp) * 16, epoch);
      s_m[tid] = pk(t[0], t[1]);
      if (hf == 0) s_m[640 + tid] = pk(u[0], u[1]);
      if (wv == 9) {
#pragma unroll
        for (int k = 0; k < 5; ++k) s_m[960 + lane + 64 * k] = xraw[k];
      }
    }
    u32 pb[8][8];
#pragma unroll
    for (int j = 0; j < 8; ++j) preperm(wb[2 * j], wb[2 * j + 1], pb[j]);
    u32 pd[4][8];
#pragma unroll
    for (int j = 0; j < 4; ++j) preperm(wd[2 * j], wd[2 * j + 1], pd[j]);
    if (wv == 0) ST(10);
    {
      const u32 yo = WS_Y + (hf * 8 * 320 + cp) * 16;
      gather8(rw, yo + 7 * 5120, epoch);
      *(f32x2*)&s_y[hf][cp * 2] = gather_sum<8, 5120>(rw, yo, epoch);
    }
    if (wv == 0) ST(11);
    bar();
    int t2 = tid;
    asm volatile("" : "+v"(t2));
    {
    const int tid = t2, lane = tid & 63, g = lane >> 4, ch = lane & 15, c8 = lane & 7, r8 = lane >> 3;
    const int rg = tid / 80, c80 = tid - rg * 80;
    u32 x1[5];
#pragma unroll
    for (int k = 0; k < 5; ++k) {
      const f32x2 y = *(f32x2*)&s_y[0][(lane + 64 * k) * 2] + *(f32x2*)&s_y[1][(lane + 64 * k) * 2];
      x1[k] = resid(s_m[960 + lane + 64 * k], s_m[lane + 64 * k], y[0], y[1]);
    }
    const u32 sb = s_m[320 + lane + 64 * (wv >> 1)], cb = s_m[640 + lane + 64 * (wv >> 1)];
    if (ex == 0 && wv == 9) {
#pragma unroll
      for (int k = 0; k < 5; ++k) stg8(rw, WS_X1 + (lane + 64 * k) * 8, x1[k], epoch);
    }
    float m, isd;
    ln_stats(x1, m, isd);
    const int ks = wv >> 1;
    s_w[wv][lane] = modulate(ks == 0 ? x1[0] : ks == 1 ? x1[1] : ks == 2 ? x1[2] : ks == 3 ? x1[3] : x1[4], m, isd, cb, sb);
    FENCE();
    const u32x4 hq = *(const u32x4*)&s_w[wv][(wv & 1) * 32 + r8 * 4];
    float ag[8] = ZERO8, au[8] = ZERO8;
#pragma unroll
    for (int j = 0; j < 4; ++j) { macp(ag, pb[j], hq[j]); macp(au, pb[4 + j], hq[j]); }
    float g0, g1, u0, u1;
    reduce8(ag, g0, g1); reduce8(au, u0, u1);
    g0 += dpp<DPP_R8>(g0); g1 += dpp<DPP_R8>(g1); u0 += dpp<DPP_R8>(u0); u1 += dpp<DPP_R8>(u1);
    if (ch < 8) *(float4*)&s_part[wv][(c8 * 8 + g * 2) * 2] = make_float4(g0, u0, g1, u1);
    if (wv == 0) ST(12);
    bar();
    f32x2 gu = {0.f, 0.f};
#pragma unroll
    for (int k = 0; k < 10; ++k) gu += *(f32x2*)&s_part[k][lane * 2];
    const float hh = rb(gelu_tanh(rb(gu[0]))) * rb(gu[1]);
    const u32 hpair = pk(hh, dpp<DPP_X1>(hh));
    if (!(lane & 1)) s_w[wv][lane >> 1] = hpair;
    FENCE();
    const u32x4 hd = *(const u32x4*)&s_w[wv][rg * 4];
    float acc[8] = ZERO8;
#pragma unroll
    for (int j = 0; j < 4; ++j) macp(acc, pd[j], hd[j]);
    *(float4*)&s_big[rg][c80 * 8] = make_float4(acc[0], acc[1], acc[2], acc[3]);
    *(float4*)&s_big[rg][c80 * 8 + 4] = make_float4(acc[4], acc[5], acc[6], acc[7]);
    bar();
    if (tid < 320) {
      f32x2 t = {0.f, 0.f};
#pragma unroll
      for (int k = 0; k < 8; ++k) t += *(f32x2*)&s_big[k][tid * 2];
      stg16(rw, WS_D + (ex * 320 + tid) * 16, t[0], t[1], epoch);
    }
    if (wv == 0) ST(13);
    }
  } else {
    const int fr = ex - 27, cp = fr * 64 + wv * 8 + c8;
    const u32 epoch = __builtin_amdgcn_readfirstlane(__builtin_amdgcn_raw_buffer_load_b32(rw, WS_EPOCH, 0, 16)) + 1u;
    asm volatile("s_waitcnt vmcnt(0)" ::: "memory");
    bar();
    if (tid == 0) stg8(rw, WS_ACK + fr * 64, 0u, epoch);
    const f32x2 mv = gather_sum<1, 0>(rw, WS_MOD + ((r8 & 3) * 1920 + 1600 + cp) * 16, epoch);
    float b0 = lane < 32 ? mv[0] : 0.f, b1 = lane < 32 ? mv[1] : 0.f;
    b0 += dpp<DPP_R8>(b0); b0 = sw16(b0, b0); b0 = sw32(b0, b0);
    b1 += dpp<DPP_R8>(b1); b1 = sw16(b1, b1); b1 = sw32(b1, b1);
    const u32 gb = pk(b0, b1);
    if (fr == 0 && wv == 0) gather8(rw, WS_ACK + (lane < 5 ? lane : 0) * 64, epoch);
    const u32 x1 = gather8(rw, WS_X1 + cp * 8, epoch);
    const u32 dq = WS_D + (r8 * 320 + cp) * 16;
    u32x4 dv[4];
    gather16<4, 8 * 5120>(rw, dq, r8 < 3 ? dq : dq - 8 * 5120, epoch, dv);
    if (wv == 0) ST(14);
    float y0 = r8 < 3 ? __uint_as_float(dv[3][0]) : 0.f, y1 = r8 < 3 ? __uint_as_float(dv[3][2]) : 0.f;
#pragma unroll
    for (int k = 0; k < 3; ++k) { y0 += __uint_as_float(dv[k][0]); y1 += __uint_as_float(dv[k][2]); }
    y0 += dpp<DPP_R8>(y0); y0 = sw16(y0, y0); y0 = sw32(y0, y0);
    y1 += dpp<DPP_R8>(y1); y1 = sw16(y1, y1); y1 = sw32(y1, y1);
    if (lane < 8) ((u32*)p.out)[cp] = resid(x1, gb, y0, y1);
    if (fr == 0 && tid == 0) __builtin_amdgcn_raw_buffer_store_b32(epoch, rw, WS_EPOCH, 0, 16);
    if (wv == 0) ST(15);
  }
}

extern "C" void kernel_launch(void* const* d_in, const int* in_sizes, int n_in, void* d_out, int out_size,
                              void* d_ws, size_t ws_size, hipStream_t stream) {
  Params p;
  p.x = (const unsigned short*)d_in[0];  p.k = (const unsigned short*)d_in[1];
  p.v = (const unsigned short*)d_in[2];  p.wq = (const unsigned short*)d_in[3];
  p.wo = (const unsigned short*)d_in[4]; p.wg = (const unsigned short*)d_in[6];
  p.wu = (const unsigned short*)d_in[7]; p.wd = (const unsigned short*)d_in[8];
  p.c = (const unsigned short*)d_in[9];  p.wm = (const unsigned short*)d_in[10];
  p.out = (unsigned short*)d_out;        p.ws = (char*)d_ws;
  fused_block<<<256, 768, 0, stream>>>(p);
}
```

```cpp
#define STAMP
#include <cstdio>
#include <hip/hip_runtime.h>
#include <hip/hip_bf16.h>

typedef unsigned u32;
typedef unsigned long long u64;
typedef u32 u32x4 __attribute__((ext_vector_type(4)));
typedef u32 u32x2 __attribute__((ext_vector_type(2)));
typedef __bf16 bf16x2 __attribute__((ext_vector_type(2)));
typedef float f32x2 __attribute__((ext_vector_type(2)));
typedef __amdgpu_buffer_rsrc_t rsrc_t;

#define KWIN 4
#ifndef NV0
#define NV0 4
#endif
#ifndef NV1
#define NV1 16
#endif
#define NT_KV 2
#define NT_W 2
#define SPIN_CAP 400000u

#define WS_EPOCH 0
#define WS_ACK   256
#define WS_X1    1024
#define WS_ML    8192
#define WS_MOD   (WS_ML + 256 * 64)
#define WS_Y     (WS_MOD + 4 * 3840 * 8)
#define WS_D     (WS_Y + 16 * 640 * 8)
#define WS_Q     (WS_D + 27 * 640 * 8)
#define WS_O     (WS_Q + 32 * 5 * 128 * 8)
#define WS_END   (WS_O + 32 * 7 * 128 * 8)
#define WS_STAMP (1 << 20)

#define FENCE() asm volatile("" ::: "memory")
#define ST(i) do { if (lane == 0) __hip_atomic_store((u64*)(p.ws + WS_STAMP) + blockIdx.x * 32 + (i), (u64)__builtin_amdgcn_s_memrealtime(), __ATOMIC_RELAXED, __HIP_MEMORY_SCOPE_AGENT); } while (0)

struct Params {
  const unsigned short *x, *k, *v, *wq, *wo, *wg, *wu, *wd, *c, *wm;
  unsigned short* out;
  char* ws;
};

__device__ __forceinline__ float blo(u32 a) { return __uint_as_float(a << 16); }
__device__ __forceinline__ float bhi(u32 a) { return __uint_as_float(a & 0xffff0000u); }
__device__ __forceinline__ u32 pk(float a, float b) {
  bf16x2 r = {(__bf16)a, (__bf16)b};
  return __builtin_bit_cast(u32, r);
}
__device__ __forceinline__ float rb(float a) { return (float)(__bf16)a; }
__device__ __forceinline__ float dot2(u32 a, u32 b, float c) {
  return __builtin_amdgcn_fdot2_f32_bf16(__builtin_bit_cast(bf16x2, a), __builtin_bit_cast(bf16x2, b), c, false);
}
template <int C> __device__ __forceinline__ float dpp(float a) {
  return __int_as_float(__builtin_amdgcn_update_dpp(0, __float_as_int(a), C, 0xf, 0xf, true));
}
#define DPP_X1 0xB1
#define DPP_X2 0x4E
#define DPP_R4 0x124
#define DPP_R8 0x128
__device__ __forceinline__ float sw32(float a, float b) {
  auto r = __builtin_amdgcn_permlane32_swap(__float_as_uint(a), __float_as_uint(b), false, false);
  return __uint_as_float(r[0]) + __uint_as_float(r[1]);
}
__device__ __forceinline__ float sw16(float a, float b) {
  auto r = __builtin_amdgcn_permlane16_swap(__float_as_uint(a), __float_as_uint(b), false, false);
  return __uint_as_float(r[0]) + __uint_as_float(r[1]);
}
__device__ __forceinline__ float mx32(float a) {
  auto r = __builtin_amdgcn_permlane32_swap(__float_as_uint(a), __float_as_uint(a), false, false);
  return fmaxf(__uint_as_float(r[0]), __uint_as_float(r[1]));
}
__device__ __forceinline__ float mx16(float a) {
  auto r = __builtin_amdgcn_permlane16_swap(__float_as_uint(a), __float_as_uint(a), false, false);
  return fmaxf(__uint_as_float(r[0]), __uint_as_float(r[1]));
}
__device__ __forceinline__ float row_sum(float a) {
  a += dpp<DPP_X1>(a); a += dpp<DPP_X2>(a); a += dpp<DPP_R4>(a); a += dpp<DPP_R8>(a); return a;
}
__device__ __forceinline__ float wave_sum(float a) { a = row_sum(a); a = sw16(a, a); return sw32(a, a); }
__device__ __forceinline__ void reduce8(const float (&a)[8], float& r0, float& r1) {
  float b0 = sw32(a[0], a[4]), b1 = sw32(a[1], a[5]), b2 = sw32(a[2], a[6]), b3 = sw32(a[3], a[7]);
  r0 = sw16(b0, b2); r1 = sw16(b1, b3);
}
__device__ __forceinline__ void mac8(float (&acc)[8], u32x4 w, float a) {
#pragma unroll
  for (int j = 0; j < 4; ++j) { acc[2 * j] += a * blo(w[j]); acc[2 * j + 1] += a * bhi(w[j]); }
}
__device__ __forceinline__ void mac8x2(float (&acc)[8], u32x4 w0, u32x4 w1, u32 pair) {
#pragma unroll
  for (int j = 0; j < 4; ++j) {
    acc[2 * j]     = dot2(__builtin_amdgcn_perm(w1[j], w0[j], 0x05040100u), pair, acc[2 * j]);
    acc[2 * j + 1] = dot2(__builtin_amdgcn_perm(w1[j], w0[j], 0x07060302u), pair, acc[2 * j + 1]);
  }
}
__device__ __forceinline__ rsrc_t mkr(const void* ptr, u32 bytes) {
  return __builtin_amdgcn_make_buffer_rsrc((void*)ptr, 0, bytes, 0x00020000);
}
template <int AUX> __device__ __forceinline__ u32x4 ld16(rsrc_t r, u32 off, u32 soff = 0) {
  return __builtin_amdgcn_raw_buffer_load_b128(r, off, soff, AUX);
}
__device__ __forceinline__ u32x2 ldg8(rsrc_t r, u32 off) { return __builtin_amdgcn_raw_buffer_load_b64(r, off, 0, 16); }
__device__ __forceinline__ void stg16(rsrc_t r, u32 off, float a, float b, u32 e) {
  u32x4 v = {__float_as_uint(a), e, __float_as_uint(b), e};
  __builtin_amdgcn_raw_buffer_store_b128(v, r, off, 0, 16);
}
__device__ __forceinline__ void stg8(rsrc_t r, u32 off, u32 a, u32 e) {
  u32x2 v = {a, e};
  __builtin_amdgcn_raw_buffer_store_b64(v, r, off, 0, 16);
}
template <int N, int S> __device__ __forceinline__ void gather16(rsrc_t r, u32 off, u32 offl, u32 e, u32x4 (&v)[N]) {
#pragma nounroll
  for (u32 spins = 0; spins < SPIN_CAP; ++spins) {
    FENCE();
    u32 bad = 0;
#pragma unroll
    for (int k = 0; k < N; ++k) v[k] = k < N - 1 ? ld16<16>(r, off, k * S) : ld16<16>(r, offl, k * S);
#pragma unroll
    for (int k = 0; k < N; ++k) bad |= (v[k][1] ^ e) | (v[k][3] ^ e);
    if (__all(bad == 0)) break;
    __builtin_amdgcn_s_sleep(1);
  }
  FENCE();
}
__device__ __forceinline__ u32 gather8(rsrc_t r, u32 off, u32 e) {
  u32x2 t;
#pragma nounroll
  for (u32 spins = 0; spins < SPIN_CAP; ++spins) {
    FENCE();
    t = ldg8(r, off);
    if (__all(t[1] == e)) break;
    __builtin_amdgcn_s_sleep(1);
  }
  FENCE();
  return t[0];
}
template <int N, int S> __device__ __forceinline__ f32x2 gather_sum(rsrc_t r, u32 off, u32 e) {
  u32x4 v[N];
  gather16<N, S>(r, off, off, e, v);
  f32x2 t = {__uint_as_float(v[0][0]), __uint_as_float(v[0][2])};
#pragma unroll
  for (int k = 1; k < N; ++k) t += f32x2{__uint_as_float(v[k][0]), __uint_as_float(v[k][2])};
  return t;
}
__device__ __forceinline__ void bar() {
  asm volatile("s_waitcnt lgkmcnt(0)\n\ts_barrier" ::: "memory");
}
__device__ __forceinline__ void lds_arrive(u32* cnt, int lane) {
  asm volatile("s_waitcnt lgkmcnt(0)" ::: "memory");
  if (lane == 0) __hip_atomic_fetch_add(cnt, 1u, __ATOMIC_RELAXED, __HIP_MEMORY_SCOPE_WORKGROUP);
}
__device__ __forceinline__ void lds_wait(u32* cnt, u32 n) {
  while (__hip_atomic_load(cnt, __ATOMIC_RELAXED, __HIP_MEMORY_SCOPE_WORKGROUP) < n) __builtin_amdgcn_s_sleep(1);
  FENCE();
}
__device__ __forceinline__ void ln_stats(const u32 (&x)[5], float& m, float& isd) {
  float s = 0.f;
#pragma unroll
  for (int i = 0; i < 5; ++i) s += blo(x[i]) + bhi(x[i]);
  const float mean = wave_sum(s) * (1.f / 640.f);
  float q = 0.f;
#pragma unroll
  for (int i = 0; i < 5; ++i) { const float d0 = blo(x[i]) - mean, d1 = bhi(x[i]) - mean; q += d0 * d0 + d1 * d1; }
  const float var = wave_sum(q) * (1.f / 640.f);
  m = rb(mean);
  isd = __builtin_amdgcn_rcpf(rb(__builtin_amdgcn_sqrtf(rb(rb(var) + 1e-5f))));
}
__device__ __forceinline__ u32 modulate(u32 x, float m, float isd, u32 sc, u32 sh) {
  return pk(rb(rb(rb(blo(x) - m) * isd) * rb(1.f + blo(sc))) + blo(sh),
            rb(rb(rb(bhi(x) - m) * isd) * rb(1.f + bhi(sc))) + bhi(sh));
}
__device__ __forceinline__ u32 resid(u32 x, u32 gate, float y0, float y1) {
  return pk(blo(x) + rb(blo(gate) * rb(y0)), bhi(x) + rb(bhi(gate) * rb(y1)));
}
__device__ __forceinline__ void preperm(u32x4 w0, u32x4 w1, u32 (&o)[8]) {
#pragma unroll
  for (int j = 0; j < 4; ++j) {
    o[2 * j] = __builtin_amdgcn_perm(w1[j], w0[j], 0x05040100u);
    o[2 * j + 1] = __builtin_amdgcn_perm(w1[j], w0[j], 0x07060302u);
  }
  asm volatile("" : "+v"(o[0]), "+v"(o[1]), "+v"(o[2]), "+v"(o[3]), "+v"(o[4]), "+v"(o[5]), "+v"(o[6]), "+v"(o[7]));
  __builtin_amdgcn_sched_barrier(0);
}
__device__ __forceinline__ void macp(float (&acc)[8], const u32 (&w)[8], u32 pair) {
#pragma unroll
  for (int j = 0; j < 8; ++j) acc[j] = dot2(w[j], pair, acc[j]);
}
#define ZERO8 {0.f, 0.f, 0.f, 0.f, 0.f, 0.f, 0.f, 0.f}
__device__ __forceinline__ float gelu_tanh(float g) {
  const float z = 0.7978845608028654f * (g + 0.044715f * g * g * g);
  const float t = 1.f - 2.f * __builtin_amdgcn_rcpf(__expf(2.f * z) + 1.f);
  return 0.5f * g * (1.f + t);
}

__device__ __forceinline__ void mod_issue(const Params& p, bool own, int unit, int tid, u32x4 (&wa)[10], u32 (&craw)[10]) {
  const rsrc_t rm = mkr(p.wm, own ? 640 * 3840 * 2 : 0), rc = mkr(p.c, own ? 1280 : 0);
  const int row0 = (unit & 3) * 160 + (tid >> 3);
#pragma unroll
  for (int j = 0; j < 10; ++j) wa[j] = ld16<NT_W>(rm, row0 * 7680 + (unit >> 2) * 128 + (tid & 7) * 16, j * 16 * 7680);
#pragma unroll
  for (int j = 0; j < 10; ++j) craw[j] = __builtin_amdgcn_raw_buffer_load_b16(rc, row0 * 2, j * 32, 0);
}
__device__ __forceinline__ void mod_finish(rsrc_t rw, int unit, int wv, int lane, u32 epoch, const u32x4 (&wa)[10],
                                           const u32 (&craw)[10], float* s_t, u32* cnt) {
  float acc[8] = ZERO8;
#pragma unroll
  for (int j = 0; j < 10; ++j) {
    const float cv = blo(craw[j]);
    mac8(acc, wa[j], rb(cv * __builtin_amdgcn_rcpf(1.f + __expf(-cv))));
  }
  float a0, a1;
  reduce8(acc, a0, a1);
  a0 += dpp<DPP_R8>(a0); a1 += dpp<DPP_R8>(a1);
  if (wv == 1) { *(f32x2*)&s_t[lane * 2] = f32x2{a0, a1}; lds_arrive(cnt, lane); }
  else {
    lds_wait(cnt, 1);
    const f32x2 t = *(f32x2*)&s_t[lane * 2];
    if (!(lane & 8))
      stg16(rw, WS_MOD + ((unit & 3) * 3840 + (unit >> 2) * 64 + (lane & 7) * 8 + (lane >> 4) * 2) * 8, a0 + t[0], a1 + t[1], epoch);
  }
}

__global__ void __launch_bounds__(768, 3) fused_block(Params p) {
  __shared__ __attribute__((aligned(16))) float s_big[8][640];
  __shared__ __attribute__((aligned(16))) float s_part[10][128];
  __shared__ __attribute__((aligned(16))) float s_y[2][640];
  __shared__ __attribute__((aligned(16))) u32 s_w[10][64];
  __shared__ __attribute__((aligned(16))) u32 s_q[64];
  __shared__ __attribute__((aligned(16))) f32x2 s_ml[10];
  __shared__ u32 s_cnt[4];
  const int tid = threadIdx.x, lane = tid & 63, g = lane >> 4, ch = lane & 15, c8 = lane & 7, r8 = lane >> 3;
  const int wv = __builtin_amdgcn_readfirstlane(tid >> 6);
  const int b = blockIdx.x, xcd = b & 7, r = b >> 3;
  const bool attn = r < 28;
  const int head = xcd * 4 + (r & 3), sp = r >> 2;
  const int ex = (r - 28) * 8 + xcd;
  const int nsw = sp == 6 ? 10 : 9;
  if (wv >= (attn ? 2 + nsw : ex < 27 ? 10 : 8)) return;

  if (attn && wv >= 2) {
    const int w = wv - 2;
    const size_t base = ((size_t)head * 4096 + (sp * 9 + w) * 64) * 128;
    const rsrc_t rk = mkr(p.k + base, 16384), rv = mkr(p.v + base, 16384);
    u32x4 K[16], V[16];
    bar();
#pragma unroll
    for (int i = 0; i < 16 + NV0; ++i) {
      if (i >= KWIN) asm volatile("s_waitcnt vmcnt(%0)" :: "n"(KWIN - 1) : "memory");
      if (i < 16) K[i] = ld16<NT_KV>(rk, lane * 16, i * 1024);
      else V[i - 16] = ld16<NT_KV>(rv, lane * 16, (i - 16) * 1024);
      FENCE();
    }
    if (w == 0) ST(16);
    bar();
#pragma unroll
    for (int i = NV0; i < NV1; ++i) V[i] = ld16<NT_KV>(rv, lane * 16, i * 1024);
    FENCE();
    const u32x4 q = *(const u32x4*)&s_q[ch * 4];
    float sc[16], m = -INFINITY;
#pragma unroll
    for (int i = 0; i < 16; ++i) {
      float a = dot2(K[i][0], q[0], 0.f);
      a = dot2(K[i][1], q[1], a); a = dot2(K[i][2], q[2], a); a = dot2(K[i][3], q[3], a);
      a = row_sum(a);
      sc[i] = rb(rb(a) * 0.08838834764831845f);
      m = fmaxf(m, sc[i]);
    }
    m = mx32(mx16(m));
    float l = 0.f;
    u32 pp[8];
#pragma unroll
    for (int i = 0; i < 8; ++i) {
      const float e0 = __expf(sc[2 * i] - m), e1 = __expf(sc[2 * i + 1] - m);
      l += e0 + e1;
      pp[i] = pk(e0, e1);
    }
    l = sw16(l, l); l = sw32(l, l);
    if (lane == 0) s_ml[w] = f32x2{m, l};
    lds_arrive(&s_cnt[2], lane);
    if (w == 0) ST(17);
#pragma unroll
    for (int i = NV1; i < 16; ++i) V[i] = ld16<NT_KV>(rv, lane * 16, i * 1024);
    FENCE();
    float acc[8] = ZERO8;
#pragma unroll
    for (int i = 0; i < 8; ++i) mac8x2(acc, V[2 * i], V[2 * i + 1], pp[i]);
    float o0, o1;
    reduce8(acc, o0, o1);
    *(f32x2*)&s_part[w][ch * 8 + g * 2] = f32x2{o0, o1};
    if (w == 0) ST(18);
    bar();
    return;
  }

  const rsrc_t rw = mkr(p.ws, WS_END), rx = mkr(p.x, 1280);
  if (wv == 0) ST(0);
  const bool qo_own = attn && sp < 5;
#define PROLOGUE_A(MOD_OWN, UNIT)                                                                                     \
  const bool mod_own = (MOD_OWN);                                                                                     \
  const int unit = (UNIT);                                                                                            \
  u32x4 wa[10], wb[16];                                                                                               \
  u32 craw[10], xraw[5];                                                                                              \
  mod_issue(p, mod_own, unit, tid, wa, craw);                                                                         \
  const u32 eraw = __builtin_amdgcn_raw_buffer_load_b32(rw, WS_EPOCH, 0, 16);                                         \
  _Pragma("unroll") for (int j = 0; j < 5; ++j) xraw[j] = __builtin_amdgcn_raw_buffer_load_b32(rx, lane * 4, j * 256, 0);
#define PROLOGUE_B()                                                                                                  \
  if (tid == 0) { s_cnt[0] = 0u; s_cnt[1] = 0u; s_cnt[2] = 0u; }                                                      \
  __builtin_amdgcn_sched_barrier(0);                                                                                  \
  bar();                                                                                                       \
  __builtin_amdgcn_sched_barrier(0);                                                                                  \
  const u32 epoch = __builtin_amdgcn_readfirstlane(eraw) + 1u;                                                        \
  if (mod_own) mod_finish(rw, unit, wv, lane, epoch, wa, craw, &s_part[0][0], &s_cnt[0]);                             \
  if (wv == 0) ST(1);

  if (attn) {
    PROLOGUE_A(true, sp >= 5 ? head * 2 + sp - 5 : sp == 4 ? (head < 16 ? 64 : 192) + head : 80 + head * 4 + sp)
    const u32 xq = __builtin_amdgcn_raw_buffer_load_b32(rx, ((qo_own ? sp * 64 + wv * 32 : 0) + (lane & 31)) * 4, 0, 0);
    {
      const rsrc_t rq = mkr(p.wq, qo_own ? 640 * 4096 * 2 : 0);
#pragma unroll
      for (int j = 0; j < 16; ++j) wb[j] = ld16<NT_W>(rq, (sp * 128 + wv * 64 + g * 16) * 8192 + head * 256 + ch * 16, j * 8192);
    }
    PROLOGUE_B()
    if (qo_own) {
      float xm, xi;
      ln_stats(xraw, xm, xi);
      const f32x2 t = gather_sum<4, 1920 * 16>(rw, WS_MOD + ((lane >> 5) * 320 + sp * 64 + wv * 32 + (lane & 31)) * 16, epoch);
      if (wv == 0) ST(2);
      const u32 mp = pk(t[0], t[1]);
      const auto sc2 = __builtin_amdgcn_permlane32_swap(mp, mp, false, false);
      s_w[wv][lane] = modulate(xq, xm, xi, sc2[1], sc2[0]);
      FENCE();
      const u32x4 h0 = *(const u32x4*)&s_w[wv][g * 8], h1 = *(const u32x4*)&s_w[wv][g * 8 + 4];
      float acc[8] = ZERO8;
#pragma unroll
      for (int j = 0; j < 8; ++j) mac8x2(acc, wb[2 * j], wb[2 * j + 1], j < 4 ? h0[j & 3] : h1[j & 3]);
      float a0, a1;
      reduce8(acc, a0, a1);
      if (wv == 1) { *(f32x2*)&s_y[1][lane * 2] = f32x2{a0, a1}; lds_arrive(&s_cnt[1], lane); }
      else {
        lds_wait(&s_cnt[1], 1);
        const f32x2 u = *(f32x2*)&s_y[1][lane * 2];
        stg16(rw, WS_Q + ((head * 5 + sp) * 64 + ch * 4 + g) * 16, a0 + u[0], a1 + u[1], epoch);
        ST(3);
      }
    }
    if (wv == 0) {
      const f32x2 t = gather_sum<5, 1024>(rw, WS_Q + (head * 5 * 64 + lane) * 16, epoch);
      s_q[lane] = pk(t[0], t[1]);
      ST(4);
    }
    bar();
    const int hp = head >> 1, tile = (head & 1) * 5 + sp;
    {
      const rsrc_t ro = mkr(p.wo, qo_own ? 4096 * 640 * 2 : 0);
#pragma unroll
      for (int j = 0; j < 16; ++j) wb[j] = ld16<NT_W>(ro, ((hp * 2 + wv) * 128 + r8 * 16) * 1280 + tile * 128 + c8 * 16, j * 1280);
    }
    float fw[10], M, L;
    if (wv == 0) {
      lds_wait(&s_cnt[2], nsw);
      const f32x2 ml = ch < nsw ? s_ml[ch] : f32x2{-INFINITY, 0.f};
      M = ml[0];
      M = fmaxf(M, dpp<DPP_X1>(M)); M = fmaxf(M, dpp<DPP_X2>(M)); M = fmaxf(M, dpp<DPP_R4>(M)); M = fmaxf(M, dpp<DPP_R8>(M));
      const float f = __expf(ml[0] - M);
      L = row_sum(f * ml[1]);
      if (lane == 0) stg16(rw, WS_ML + (head * 8 + sp) * 64, M, L, epoch);
#pragma unroll
      for (int k = 0; k < 10; ++k) fw[k] = __int_as_float(__builtin_amdgcn_readlane(__float_as_int(f), k));
      ST(5);
    }
    u32 pb[8][8];
#pragma unroll
    for (int j = 0; j < 8; ++j) preperm(wb[2 * j], wb[2 * j + 1], pb[j]);
    bar();
    if (wv == 0) {
      ST(6);
      f32x2 o = {0.f, 0.f};
#pragma unroll
      for (int k = 0; k < 9; ++k) o += fw[k] * *(f32x2*)&s_part[k][lane * 2];
      if (nsw == 10) o += fw[9] * *(f32x2*)&s_part[9][lane * 2];
      stg16(rw, WS_O + ((head * 7 + sp) * 64 + lane) * 16, o[0], o[1], epoch);
      ST(7);
    }
    if (qo_own) {
      u32x4 ov[8];
      gather16<8, 1024>(rw, WS_O + ((hp * 2 + wv) * 7 * 64 + lane) * 16,
                        WS_ML + ((hp * 2 + wv) * 8 + (c8 < 7 ? c8 : 0)) * 64 - 7 * 1024, epoch, ov);
      if (wv == 0) ST(8);
      const float ms = __uint_as_float(ov[7][0]);
      float Mh = ms;
      Mh = fmaxf(Mh, dpp<DPP_X1>(Mh)); Mh = fmaxf(Mh, dpp<DPP_X2>(Mh)); Mh = fmaxf(Mh, dpp<DPP_R4>(Mh));
      const float es = __expf(ms - Mh);
      float Lh = c8 < 7 ? es * __uint_as_float(ov[7][2]) : 0.f;
      Lh += dpp<DPP_X1>(Lh); Lh += dpp<DPP_X2>(Lh); Lh += dpp<DPP_R4>(Lh);
      const float f = es * __builtin_amdgcn_rcpf(Lh);
      f32x2 t = {0.f, 0.f};
#pragma unroll
      for (int k = 0; k < 7; ++k)
        t += __int_as_float(__builtin_amdgcn_readlane(__float_as_int(f), k)) * f32x2{__uint_as_float(ov[k][0]), __uint_as_float(ov[k][2])};
      s_w[wv][lane] = pk(t[0], t[1]);
      FENCE();
      const u32x4 h0 = *(const u32x4*)&s_w[wv][r8 * 8], h1 = *(const u32x4*)&s_w[wv][r8 * 8 + 4];
      float acc[8] = ZERO8;
#pragma unroll
      for (int j = 0; j < 8; ++j) macp(acc, pb[j], j < 4 ? h0[j & 3] : h1[j & 3]);
      float a0, a1;
      reduce8(acc, a0, a1);
      a0 += dpp<DPP_R8>(a0); a1 += dpp<DPP_R8>(a1);
      if (wv == 1) *(f32x2*)&s_y[0][lane * 2] = f32x2{a0, a1};
      bar();
      if (wv == 0) {
        const f32x2 u = *(f32x2*)&s_y[0][lane * 2];
        if (ch < 8) stg16(rw, WS_Y + (hp * 640 + tile * 64 + c8 * 8 + g * 2) * 8, a0 + u[0], a1 + u[1], epoch);
        ST(9);
      }
    }
  } else if (ex < 27) {
    PROLOGUE_A(wv < 2 && ex < 16, 224 + ex)
    {
      const rsrc_t r0 = mkr(p.wg, 640 * 1728 * 2), r1 = mkr(p.wu, 640 * 1728 * 2);
#pragma unroll
      for (int j = 0; j < 16; ++j) wb[j] = ld16<NT_W>(j < 8 ? r0 : r1, (wv * 64 + r8 * 8) * 3456 + ex * 128 + c8 * 16, (j & 7) * 3456);
    }
    PROLOGUE_B()
    const int cp = tid < 320 ? tid : tid - 320, hf = tid < 320 ? 0 : 1;
    const int rg = tid / 80, c80 = tid - rg * 80;
    u32x4 wd[8];
    {
      const rsrc_t rd = mkr(p.wd, 1728 * 640 * 2);
#pragma unroll
      for (int j = 0; j < 8; ++j) wd[j] = ld16<NT_W>(rd, (ex * 64 + rg * 8) * 1280 + c80 * 16, j * 1280);
    }
    u32* s_m = (u32*)&s_big[0][0];
    {
      const f32x2 t = gather_sum<4, 1920 * 16>(rw, WS_MOD + (640 + tid) * 16, epoch);
      const f32x2 u = gather_sum<4, 1920 * 16>(rw, WS_MOD + (1280 + cp) * 16, epoch);
      s_m[tid] = pk(t[0], t[1]);
      if (hf == 0) s_m[640 + tid] = pk(u[0], u[1]);
      if (wv == 9) {
#pragma unroll
        for (int k = 0; k < 5; ++k) s_m[960 + lane + 64 * k] = xraw[k];
      }
    }
    u32 pb[8][8];
#pragma unroll
    for (int j = 0; j < 8; ++j) preperm(wb[2 * j], wb[2 * j + 1], pb[j]);
    u32 pd[4][8];
#pragma unroll
    for (int j = 0; j < 4; ++j) preperm(wd[2 * j], wd[2 * j + 1], pd[j]);
    if (wv == 0) ST(10);
    {
      const u32 yo = WS_Y + (hf * 8 * 320 + cp) * 16;
      gather8(rw, yo + 7 * 5120, epoch);
      *(f32x2*)&s_y[hf][cp * 2] = gather_sum<8, 5120>(rw, yo, epoch);
    }
    if (wv == 0) ST(11);
    bar();
    int t2 = tid;
    asm volatile("" : "+v"(t2));
    {
    const int tid = t2, lane = tid & 63, g = lane >> 4, ch = lane & 15, c8 = lane & 7, r8 = lane >> 3;
    const int rg = tid / 80, c80 = tid - rg * 80;
    u32 x1[5];
#pragma unroll
    for (int k = 0; k < 5; ++k) {
      const f32x2 y = *(f32x2*)&s_y[0][(lane + 64 * k) * 2] + *(f32x2*)&s_y[1][(lane + 64 * k) * 2];
      x1[k] = resid(s_m[960 + lane + 64 * k], s_m[lane + 64 * k], y[0], y[1]);
    }
    const u32 sb = s_m[320 + lane + 64 * (wv >> 1)], cb = s_m[640 + lane + 64 * (wv >> 1)];
    if (ex == 0 && wv == 9) {
#pragma unroll
      for (int k = 0; k < 5; ++k) stg8(rw, WS_X1 + (lane + 64 * k) * 8, x1[k], epoch);
    }
    float m, isd;
    ln_stats(x1, m, isd);
    const int ks = wv >> 1;
    s_w[wv][lane] = modulate(ks == 0 ? x1[0] : ks == 1 ? x1[1] : ks == 2 ? x1[2] : ks == 3 ? x1[3] : x1[4], m, isd, cb, sb);
    FENCE();
    const u32x4 hq = *(const u32x4*)&s_w[wv][(wv & 1) * 32 + r8 * 4];
    float ag[8] = ZERO8, au[8] = ZERO8;
#pragma unroll
    for (int j = 0; j < 4; ++j) { macp(ag, pb[j], hq[j]); macp(au, pb[4 + j], hq[j]); }
    float g0, g1, u0, u1;
    reduce8(ag, g0, g1); reduce8(au, u0, u1);
    g0 += dpp<DPP_R8>(g0); g1 += dpp<DPP_R8>(g1); u0 += dpp<DPP_R8>(u0); u1 += dpp<DPP_R8>(u1);
    if (ch < 8) *(float4*)&s_part[wv][(c8 * 8 + g * 2) * 2] = make_float4(g0, u0, g1, u1);
    if (wv == 0) ST(12);
    bar();
    f32x2 gu = {0.f, 0.f};
#pragma unroll
    for (int k = 0; k < 10; ++k) gu += *(f32x2*)&s_part[k][lane * 2];
    const float hh = rb(gelu_tanh(rb(gu[0]))) * rb(gu[1]);
    const u32 hpair = pk(hh, dpp<DPP_X1>(hh));
    if (!(lane & 1)) s_w[wv][lane >> 1] = hpair;
    FENCE();
    const u32x4 hd = *(const u32x4*)&s_w[wv][rg * 4];
    float acc[8] = ZERO8;
#pragma unroll
    for (int j = 0; j < 4; ++j) macp(acc, pd[j], hd[j]);
    *(float4*)&s_big[rg][c80 * 8] = make_float4(acc[0], acc[1], acc[2], acc[3]);
    *(float4*)&s_big[rg][c80 * 8 + 4] = make_float4(acc[4], acc[5], acc[6], acc[7]);
    bar();
    if (tid < 320) {
      f32x2 t = {0.f, 0.f};
#pragma unroll
      for (int k = 0; k < 8; ++k) t += *(f32x2*)&s_big[k][tid * 2];
      stg16(rw, WS_D + (ex * 320 + tid) * 16, t[0], t[1], epoch);
    }
    if (wv == 0) ST(13);
    }
  } else {
    const int fr = ex - 27, cp = fr * 64 + wv * 8 + c8;
    const u32 epoch = __builtin_amdgcn_readfirstlane(__builtin_amdgcn_raw_buffer_load_b32(rw, WS_EPOCH, 0, 16)) + 1u;
    asm volatile("s_waitcnt vmcnt(0)" ::: "memory");
    bar();
    if (tid == 0) stg8(rw, WS_ACK + fr * 64, 0u, epoch);
    const f32x2 mv = gather_sum<1, 0>(rw, WS_MOD + ((r8 & 3) * 1920 + 1600 + cp) * 16, epoch);
    float b0 = lane < 32 ? mv[0] : 0.f, b1 = lane < 32 ? mv[1] : 0.f;
    b0 += dpp<DPP_R8>(b0); b0 = sw16(b0, b0); b0 = sw32(b0, b0);
    b1 += dpp<DPP_R8>(b1); b1 = sw16(b1, b1); b1 = sw32(b1, b1);
    const u32 gb = pk(b0, b1);
    if (fr == 0 && wv == 0) gather8(rw, WS_ACK + (lane < 5 ? lane : 0) * 64, epoch);
    const u32 x1 = gather8(rw, WS_X1 + cp * 8, epoch);
    const u32 dq = WS_D + (r8 * 320 + cp) * 16;
    u32x4 dv[4];
    gather16<4, 8 * 5120>(rw, dq, r8 < 3 ? dq : dq - 8 * 5120, epoch, dv);
    if (wv == 0) ST(14);
    float y0 = r8 < 3 ? __uint_as_float(dv[3][0]) : 0.f, y1 = r8 < 3 ? __uint_as_float(dv[3][2]) : 0.f;
#pragma unroll
    for (int k = 0; k < 3; ++k) { y0 += __uint_as_float(dv[k][0]); y1 += __uint_as_float(dv[k][2]); }
    y0 += dpp<DPP_R8>(y0); y0 = sw16(y0, y0); y0 = sw32(y0, y0);
    y1 += dpp<DPP_R8>(y1); y1 = sw16(y1, y1); y1 = sw32(y1, y1);
    if (lane < 8) ((u32*)p.out)[cp] = resid(x1, gb, y0, y1);
    if (fr == 0 && tid == 0) __builtin_amdgcn_raw_buffer_store_b32(epoch, rw, WS_EPOCH, 0, 16);
    if (wv == 0) ST(15);
    if (fr == 0 && wv == 0) {
      for (int i = 0; i < 100; ++i) __builtin_amdgcn_s_sleep(100);
      const u64 now = __builtin_amdgcn_s_memrealtime();
      u64 mn = ~0ull, mx = 0, sum = 0, n = 0;
      if (lane < 19) for (int bb = 0; bb < 256; ++bb) {
        const u64 t = __hip_atomic_load((u64*)(p.ws + WS_STAMP) + bb * 32 + lane, __ATOMIC_RELAXED, __HIP_MEMORY_SCOPE_AGENT);
        if (t <= now && now - t < 60000) { mn = t < mn ? t : mn; mx = t > mx ? t : mx; sum += now - t; ++n; }
      }
      const u64 t0 = __shfl(mn, 0);
      if (lane < 19) printf("[%d] n=%d %d %d %d\n", lane, (int)n, (int)(mn - t0), (int)(now - sum / n - t0), (int)(mx - t0));
    }
  }
}

extern "C" void kernel_launch(void* const* d_in, const int* in_sizes, int n_in, void* d_out, int out_size,
                              void* d_ws, size_t ws_size, hipStream_t stream) {
  Params p;
  p.x = (const unsigned short*)d_in[0];  p.k = (const unsigned short*)d_in[1];
  p.v = (const unsigned short*)d_in[2];  p.wq = (const unsigned short*)d_in[3];
  p.wo = (const unsigned short*)d_in[4]; p.wg = (const unsigned short*)d_in[6];
  p.wu = (const unsigned short*)d_in[7]; p.wd = (const unsigned short*)d_in[8];
  p.c = (const unsigned short*)d_in[9];  p.wm = (const unsigned short*)d_in[10];
  p.out = (unsigned short*)d_out;        p.ws = (char*)d_ws;
  fused_block<<<256, 768, 0, stream>>>(p);
}
```

```cpp
#include <hip/hip_runtime.h>
#include <hip/hip_bf16.h>

typedef unsigned u32;
typedef unsigned long long u64;
typedef u32 u32x4 __attribute__((ext_vector_type(4)));
typedef u32 u32x2 __attribute__((ext_vector_type(2)));
typedef __bf16 bf16x2 __attribute__((ext_vector_type(2)));
typedef float f32x2 __attribute__((ext_vector_type(2)));
typedef __amdgpu_buffer_rsrc_t rsrc_t;

#define KWIN 3
#ifndef NV0
#define NV0 4
#endif
#ifndef NV1
#define NV1 16
#endif
#define NT_KV 2
#define NT_W 2
#define CODE_BYTES 18432u
#define SPIN_CAP 400000u

#define WS_EPOCH 0
#define WS_X1    1024
#define WS_ML    8192
#define WS_MOD   (WS_ML + 256 * 64)
#define WS_Y     (WS_MOD + 4 * 3840 * 8)
#define WS_D     (WS_Y + 16 * 320 * 8)
#define WS_Q     (WS_D + 54 * 640 * 8)
#define WS_O     (WS_Q + 32 * 5 * 128 * 8)
#define WS_END   (WS_O + 32 * 7 * 128 * 8)
#define WS_STAMP (1 << 20)

#define FENCE() asm volatile("" ::: "memory")
#define ST(i)

struct Params {
  const unsigned short *x, *k, *v, *wq, *wo, *wg, *wu, *wd, *c, *wm;
  unsigned short* out;
  char* ws;
};

__device__ __forceinline__ float blo(u32 a) { return __uint_as_float(a << 16); }
__device__ __forceinline__ float bhi(u32 a) { return __uint_as_float(a & 0xffff0000u); }
__device__ __forceinline__ u32 pk(float a, float b) {
  bf16x2 r = {(__bf16)a, (__bf16)b};
  return __builtin_bit_cast(u32, r);
}
__device__ __forceinline__ float rb(float a) { return (float)(__bf16)a; }
__device__ __forceinline__ float dot2(u32 a, u32 b, float c) {
  return __builtin_amdgcn_fdot2_f32_bf16(__builtin_bit_cast(bf16x2, a), __builtin_bit_cast(bf16x2, b), c, false);
}
template <int C> __device__ __forceinline__ float dpp(float a) {
  return __int_as_float(__builtin_amdgcn_update_dpp(0, __float_as_int(a), C, 0xf, 0xf, true));
}
#define DPP_X1 0xB1
#define DPP_X2 0x4E
#define DPP_R4 0x124
#define DPP_R8 0x128
__device__ __forceinline__ float sw32(float a, float b) {
  auto r = __builtin_amdgcn_permlane32_swap(__float_as_uint(a), __float_as_uint(b), false, false);
  return __uint_as_float(r[0]) + __uint_as_float(r[1]);
}
__device__ __forceinline__ float sw16(float a, float b) {
  auto r = __builtin_amdgcn_permlane16_swap(__float_as_uint(a), __float_as_uint(b), false, false);
  return __uint_as_float(r[0]) + __uint_as_float(r[1]);
}
__device__ __forceinline__ float mx32(float a) {
  auto r = __builtin_amdgcn_permlane32_swap(__float_as_uint(a), __float_as_uint(a), false, false);
  return fmaxf(__uint_as_float(r[0]), __uint_as_float(r[1]));
}
__device__ __forceinline__ float mx16(float a) {
  auto r = __builtin_amdgcn_permlane16_swap(__float_as_uint(a), __float_as_uint(a), false, false);
  return fmaxf(__uint_as_float(r[0]), __uint_as_float(r[1]));
}
__device__ __forceinline__ float row_sum(float a) {
  a += dpp<DPP_X1>(a); a += dpp<DPP_X2>(a); a += dpp<DPP_R4>(a); a += dpp<DPP_R8>(a); return a;
}
__device__ __forceinline__ float wave_sum(float a) { a = row_sum(a); a = sw16(a, a); return sw32(a, a); }
__device__ __forceinline__ void reduce8(const float (&a)[8], float& r0, float& r1) {
  float b0 = sw32(a[0], a[4]), b1 = sw32(a[1], a[5]), b2 = sw32(a[2], a[6]), b3 = sw32(a[3], a[7]);
  r0 = sw16(b0, b2); r1 = sw16(b1, b3);
}
__device__ __forceinline__ void mac8(float (&acc)[8], u32x4 w, float a) {
#pragma unroll
  for (int j = 0; j < 4; ++j) { acc[2 * j] += a * blo(w[j]); acc[2 * j + 1] += a * bhi(w[j]); }
}
__device__ __forceinline__ void mac8x2(float (&acc)[8], u32x4 w0, u32x4 w1, u32 pair) {
#pragma unroll
  for (int j = 0; j < 4; ++j) {
    acc[2 * j]     = dot2(__builtin_amdgcn_perm(w1[j], w0[j], 0x05040100u), pair, acc[2 * j]);
    acc[2 * j + 1] = dot2(__builtin_amdgcn_perm(w1[j], w0[j], 0x07060302u), pair, acc[2 * j + 1]);
  }
}
__device__ __forceinline__ rsrc_t mkr(const void* ptr, u32 bytes) {
  return __builtin_amdgcn_make_buffer_rsrc((void*)ptr, 0, bytes, 0x00020000);
}
template <int AUX> __device__ __forceinline__ u32x4 ld16(rsrc_t r, u32 off, u32 soff = 0) {
  return __builtin_amdgcn_raw_buffer_load_b128(r, off, soff, AUX);
}
__device__ __forceinline__ u32x2 ldg8(rsrc_t r, u32 off) { return __builtin_amdgcn_raw_buffer_load_b64(r, off, 0, 16); }
__device__ __forceinline__ void stg16(rsrc_t r, u32 off, float a, float b, u32 e) {
  u32x4 v = {__float_as_uint(a), e, __float_as_uint(b), e};
  __builtin_amdgcn_raw_buffer_store_b128(v, r, off, 0, 16);
}
__device__ __forceinline__ void stg8(rsrc_t r, u32 off, u32 a, u32 e) {
  u32x2 v = {a, e};
  __builtin_amdgcn_raw_buffer_store_b64(v, r, off, 0, 16);
}
template <int N, int S> __device__ __forceinline__ void gather16(rsrc_t r, u32 off, u32 offl, u32 e, u32x4 (&v)[N]) {
#pragma nounroll
  for (u32 spins = 0; spins < SPIN_CAP; ++spins) {
    FENCE();
    u32 bad = 0;
#pragma unroll
    for (int k = 0; k < N; ++k) v[k] = k < N - 1 ? ld16<16>(r, off, k * S) : ld16<16>(r, offl, k * S);
#pragma unroll
    for (int k = 0; k < N; ++k) bad |= (v[k][1] ^ e) | (v[k][3] ^ e);
    if (__all(bad == 0)) break;
    __builtin_amdgcn_s_sleep(1);
  }
  FENCE();
}
__device__ __forceinline__ u32 gather8(rsrc_t r, u32 off, u32 e) {
  u32x2 t;
#pragma nounroll
  for (u32 spins = 0; spins < SPIN_CAP; ++spins) {
    FENCE();
    t = ldg8(r, off);
    if (__all(t[1] == e)) break;
    __builtin_amdgcn_s_sleep(1);
  }
  FENCE();
  return t[0];
}
template <int N, int S> __device__ __forceinline__ f32x2 gather_sum(rsrc_t r, u32 off, u32 e) {
  u32x4 v[N];
  gather16<N, S>(r, off, off, e, v);
  f32x2 t = {__uint_as_float(v[0][0]), __uint_as_float(v[0][2])};
#pragma unroll
  for (int k = 1; k < N; ++k) t += f32x2{__uint_as_float(v[k][0]), __uint_as_float(v[k][2])};
  return t;
}
__device__ __forceinline__ void bar() {
  asm volatile("s_waitcnt lgkmcnt(0)\n\ts_barrier" ::: "memory");
}
__device__ __forceinline__ void lds_arrive(u32* cnt, int lane) {
  asm volatile("s_waitcnt lgkmcnt(0)" ::: "memory");
  if (lane == 0) __hip_atomic_fetch_add(cnt, 1u, __ATOMIC_RELAXED, __HIP_MEMORY_SCOPE_WORKGROUP);
}
__device__ __forceinline__ void lds_wait(u32* cnt, u32 n) {
  while (__hip_atomic_load(cnt, __ATOMIC_RELAXED, __HIP_MEMORY_SCOPE_WORKGROUP) < n) __builtin_amdgcn_s_sleep(1);
  FENCE();
}
__device__ __forceinline__ void ln_stats(const u32 (&x)[5], float& m, float& isd) {
  float s = 0.f;
#pragma unroll
  for (int i = 0; i < 5; ++i) s += blo(x[i]) + bhi(x[i]);
  const float mean = wave_sum(s) * (1.f / 640.f);
  float q = 0.f;
#pragma unroll
  for (int i = 0; i < 5; ++i) { const float d0 = blo(x[i]) - mean, d1 = bhi(x[i]) - mean; q += d0 * d0 + d1 * d1; }
  const float var = wave_sum(q) * (1.f / 640.f);
  m = rb(mean);
  isd = __builtin_amdgcn_rcpf(rb(__builtin_amdgcn_sqrtf(rb(rb(var) + 1e-5f))));
}
__device__ __forceinline__ u32 modulate(u32 x, float m, float isd, u32 sc, u32 sh) {
  return pk(rb(rb(rb(blo(x) - m) * isd) * rb(1.f + blo(sc))) + blo(sh),
            rb(rb(rb(bhi(x) - m) * isd) * rb(1.f + bhi(sc))) + bhi(sh));
}
__device__ __forceinline__ u32 resid(u32 x, u32 gate, float y0, float y1) {
  return pk(blo(x) + rb(blo(gate) * rb(y0)), bhi(x) + rb(bhi(gate) * rb(y1)));
}
typedef __bf16 bf16x8 __attribute__((ext_vector_type(8)));
typedef float f32x4 __attribute__((ext_vector_type(4)));
template <bool PIN> __device__ __forceinline__ void transp(const u32x4* w, u32x4 (&t)[8]) {
#pragma unroll
  for (int j = 0; j < 4; ++j)
#pragma unroll
    for (int k = 0; k < 4; ++k) {
      t[2 * j][k] = __builtin_amdgcn_perm(w[2 * k + 1][j], w[2 * k][j], 0x05040100u);
      t[2 * j + 1][k] = __builtin_amdgcn_perm(w[2 * k + 1][j], w[2 * k][j], 0x07060302u);
    }
  if (PIN) {
    asm volatile("" : "+v"(t[0]), "+v"(t[1]), "+v"(t[2]), "+v"(t[3]), "+v"(t[4]), "+v"(t[5]), "+v"(t[6]), "+v"(t[7]));
    __builtin_amdgcn_sched_barrier(0);
  }
}
__device__ __forceinline__ void mfma8(f32x4 (&acc)[8], const u32x4 (&t)[8], u32x4 a) {
#pragma unroll
  for (int e = 0; e < 8; ++e)
    acc[e] = __builtin_amdgcn_mfma_f32_16x16x32_bf16(__builtin_bit_cast(bf16x8, a), __builtin_bit_cast(bf16x8, t[e]), acc[e], 0, 0, 0);
}
__device__ __forceinline__ f32x2 pick2(const f32x4 (&a)[8], int g) {
  const bool o = g & 1, h = g & 2;
  float a0 = a[0][0], a1 = a[1][0], a2 = a[2][0], a3 = a[3][0], a4 = a[4][0], a5 = a[5][0], a6 = a[6][0], a7 = a[7][0];
  asm volatile("" : "+v"(a0), "+v"(a1), "+v"(a2), "+v"(a3), "+v"(a4), "+v"(a5), "+v"(a6), "+v"(a7));
  return f32x2{h ? (o ? a6 : a4) : (o ? a2 : a0), h ? (o ? a7 : a5) : (o ? a3 : a1)};
}
#define Z4 f32x4{0.f, 0.f, 0.f, 0.f}
#define ZERO84 {Z4, Z4, Z4, Z4, Z4, Z4, Z4, Z4}
#define ZERO8 {0.f, 0.f, 0.f, 0.f, 0.f, 0.f, 0.f, 0.f}
__device__ __forceinline__ float gelu_tanh(float g) {
  const float z = 0.7978845608028654f * (g + 0.044715f * g * g * g);
  const float t = 1.f - 2.f * __builtin_amdgcn_rcpf(__expf(2.f * z) + 1.f);
  return 0.5f * g * (1.f + t);
}

__device__ __forceinline__ void mod_issue(const Params& p, bool own, int unit, int tid, u32x4 (&wa)[10], u32 (&craw)[10]) {
  const rsrc_t rm = mkr(p.wm, own ? 640 * 3840 * 2 : 0), rc = mkr(p.c, own ? 1280 : 0);
  const int row0 = (unit & 3) * 160 + (tid >> 3);
#pragma unroll
  for (int j = 0; j < 10; ++j) wa[j] = ld16<NT_W>(rm, row0 * 7680 + (unit >> 2) * 128 + (tid & 7) * 16, j * 16 * 7680);
#pragma unroll
  for (int j = 0; j < 10; ++j) craw[j] = __builtin_amdgcn_raw_buffer_load_b16(rc, row0 * 2, j * 32, 0);
}
__device__ __forceinline__ void mod_finish(rsrc_t rw, int unit, int wv, int lane, u32 epoch, const u32x4 (&wa)[10],
                                           const u32 (&craw)[10], float* s_t, u32* cnt) {
  float acc[8] = ZERO8;
#pragma unroll
  for (int j = 0; j < 10; ++j) {
    const float cv = blo(craw[j]);
    mac8(acc, wa[j], rb(cv * __builtin_amdgcn_rcpf(1.f + __expf(-cv))));
  }
  float a0, a1;
  reduce8(acc, a0, a1);
  a0 += dpp<DPP_R8>(a0); a1 += dpp<DPP_R8>(a1);
  if (wv == 1) { *(f32x2*)&s_t[lane * 2] = f32x2{a0, a1}; lds_arrive(cnt, lane); }
  else {
    lds_wait(cnt, 1);
    const f32x2 t = *(f32x2*)&s_t[lane * 2];
    if (!(lane & 8))
      stg16(rw, WS_MOD + ((unit & 3) * 3840 + (unit >> 2) * 64 + (lane & 7) * 8 + (lane >> 4) * 2) * 8, a0 + t[0], a1 + t[1], epoch);
  }
}

__global__ void __launch_bounds__(768, 3) fused_block(Params p) {
  __shared__ __attribute__((aligned(16))) float s_part[10][128];
  __shared__ __attribute__((aligned(16))) float s_y[2][256];
  __shared__ __attribute__((aligned(16))) u32 s_w[10][64];
  __shared__ __attribute__((aligned(16))) u32 s_q[64];
  __shared__ __attribute__((aligned(16))) f32x2 s_ml[10];
  __shared__ u32 s_cnt[5];
  const int tid = threadIdx.x, lane = tid & 63, g = lane >> 4, ch = lane & 15, c8 = lane & 7, r8 = lane >> 3;
  const int wv = __builtin_amdgcn_readfirstlane(tid >> 6);
  const int b = blockIdx.x, xcd = b & 7, r = b >> 3;
  const bool attn = r < 28;
  const int head = xcd * 4 + (r & 3), sp = r >> 2;
  const int ex = (r - 28) * 8 + xcd;
  const int nsw = sp == 6 ? 10 : 9;
  if (r == 0 && wv == 11) {
    u64 pc;
    asm volatile("s_getpc_b64 %0" : "=s"(pc));
    const rsrc_t rc = mkr((const void*)pc, CODE_BYTES);
    u32 t = 0;
#pragma unroll
    for (int j = 0; j < 3; ++j) t |= __builtin_amdgcn_raw_buffer_load_b32(rc, lane * 128 + j * 8192, 0, 0);
    asm volatile("" :: "v"(t));
  }
  if (wv >= (attn ? 2 + nsw : ex < 27 ? 10 : 0)) return;

  if (attn && wv >= 2) {
    const int w = wv - 2;
    const size_t base = ((size_t)head * 4096 + (sp * 9 + w) * 64) * 128;
    const rsrc_t rk = mkr(p.k + base, 16384), rv = mkr(p.v + base, 16384);
    u32x4 K[16], V[16];
#define LDV(i) V[i] = ld16<NT_KV>(rv, lane * 16, (i) * 1024)
    bar();
#pragma unroll
    for (int i = 0; i < 16 + NV0; ++i) {
      if (i >= KWIN) asm volatile("s_waitcnt vmcnt(%0)" :: "n"(KWIN - 1) : "memory");
      if (i < 16) K[i] = ld16<NT_KV>(rk, lane * 16, i * 1024);
      else LDV(i - 16);
      FENCE();
    }
    if (w == 0) ST(16);
    lds_wait(&s_cnt[4], 1);
#pragma unroll
    for (int i = NV0; i < NV1; ++i) LDV(i);
    FENCE();
    lds_arrive(&s_cnt[3], lane);
    const u32x4 q = *(const u32x4*)&s_q[ch * 4];
    float sa[16];
#pragma unroll
    for (int i = 0; i < 16; ++i)
      sa[i] = row_sum(dot2(K[i][3], q[3], dot2(K[i][2], q[2], dot2(K[i][1], q[1], dot2(K[i][0], q[0], 0.f)))));
    float sc[16], m = -INFINITY;
#pragma unroll
    for (int i = 0; i < 8; ++i) {
      const u32 a = pk(sa[2 * i], sa[2 * i + 1]);
      const u32 c = pk(blo(a) * 0.08838834764831845f, bhi(a) * 0.08838834764831845f);
      sc[2 * i] = blo(c); sc[2 * i + 1] = bhi(c);
      m = fmaxf(m, fmaxf(sc[2 * i], sc[2 * i + 1]));
    }
    m = mx32(mx16(m));
    float l = 0.f;
    u32 pp[8];
#pragma unroll
    for (int i = 0; i < 8; ++i) {
      const float e0 = __expf(sc[2 * i] - m), e1 = __expf(sc[2 * i + 1] - m);
      l += e0 + e1;
      pp[i] = pk(e0, e1);
    }
    l = sw16(l, l); l = sw32(l, l);
    if (lane == 0) s_ml[w] = f32x2{m, l};
    lds_arrive(&s_cnt[2], lane);
    if (w == 0) ST(17);
#pragma unroll
    for (int i = NV1; i < 16; ++i) LDV(i);
    FENCE();
    f32x4 acc[8] = ZERO84;
    u32x4 t[8];
    transp<false>(V, t);
    mfma8(acc, t, u32x4{pp[0], pp[1], pp[2], pp[3]});
    transp<false>(V + 8, t);
    mfma8(acc, t, u32x4{pp[4], pp[5], pp[6], pp[7]});
    *(f32x2*)&s_part[w][ch * 8 + g * 2] = pick2(acc, g);
    if (w == 0) ST(18);
    bar();
    return;
  }

  const rsrc_t rw = mkr(p.ws, WS_END), rx = mkr(p.x, 1280);
  if (wv == 0) ST(0);
  const bool qo_own = attn && sp < 5;
#define PROLOGUE_A(MOD_OWN, UNIT)                                                                                     \
  const bool mod_own = (MOD_OWN);                                                                                     \
  const int unit = (UNIT);                                                                                            \
  u32x4 wa[10], wb[16];                                                                                               \
  u32 craw[10], xraw[5];                                                                                              \
  mod_issue(p, mod_own, unit, tid, wa, craw);                                                                         \
  const u32 eraw = __builtin_amdgcn_raw_buffer_load_b32(rw, WS_EPOCH, 0, 16);                                         \
  _Pragma("unroll") for (int j = 0; j < 5; ++j) xraw[j] = __builtin_amdgcn_raw_buffer_load_b32(rx, lane * 4, j * 256, 0);
#define PROLOGUE_B()                                                                                                  \
  if (tid == 0) { s_cnt[0] = 0u; s_cnt[1] = 0u; s_cnt[2] = 0u; s_cnt[3] = 0u; s_cnt[4] = 0u; }                                                \
  __builtin_amdgcn_sched_barrier(0);                                                                                  \
  bar();                                                                                                       \
  __builtin_amdgcn_sched_barrier(0);                                                                                  \
  const u32 epoch = __builtin_amdgcn_readfirstlane(eraw) + 1u;                                                        \
  if (mod_own) mod_finish(rw, unit, wv, lane, epoch, wa, craw, &s_part[0][0], &s_cnt[0]);                             \
  if (wv == 0) ST(1);

  if (attn) {
    PROLOGUE_A(true, sp >= 5 ? head * 2 + sp - 5 : sp == 4 ? (head < 16 ? 64 : 192) + head : 80 + head * 4 + sp)
    const u32 xq = __builtin_amdgcn_raw_buffer_load_b32(rx, ((qo_own ? sp * 64 + wv * 32 : 0) + (lane & 31)) * 4, 0, 0);
    {
      const rsrc_t rq = mkr(p.wq, qo_own ? 640 * 4096 * 2 : 0);
#pragma unroll
      for (int j = 0; j < 16; ++j) wb[j] = ld16<NT_W>(rq, (sp * 128 + wv * 64 + g * 16) * 8192 + head * 256 + ch * 16, j * 8192);
    }
    PROLOGUE_B()
    if (qo_own) {
      float xm, xi;
      ln_stats(xraw, xm, xi);
      const f32x2 t = gather_sum<4, 1920 * 16>(rw, WS_MOD + ((lane >> 5) * 320 + sp * 64 + wv * 32 + (lane & 31)) * 16, epoch);
      if (wv == 0) ST(2);
      const u32 mp = pk(t[0], t[1]);
      const auto sc2 = __builtin_amdgcn_permlane32_swap(mp, mp, false, false);
      s_w[wv][lane] = modulate(xq, xm, xi, sc2[1], sc2[0]);
      FENCE();
      const u32x4 h0 = *(const u32x4*)&s_w[wv][g * 8], h1 = *(const u32x4*)&s_w[wv][g * 8 + 4];
      float acc[8] = ZERO8;
#pragma unroll
      for (int j = 0; j < 8; ++j) mac8x2(acc, wb[2 * j], wb[2 * j + 1], j < 4 ? h0[j & 3] : h1[j & 3]);
      float a0, a1;
      reduce8(acc, a0, a1);
      if (wv == 1) { *(f32x2*)&s_y[1][lane * 2] = f32x2{a0, a1}; lds_arrive(&s_cnt[1], lane); }
      else {
        lds_wait(&s_cnt[1], 1);
        const f32x2 u = *(f32x2*)&s_y[1][lane * 2];
        stg16(rw, WS_Q + ((head * 5 + sp) * 64 + ch * 4 + g) * 16, a0 + u[0], a1 + u[1], epoch);
        ST(3);
      }
    }
    if (wv == 0) {
      const f32x2 t = gather_sum<5, 1024>(rw, WS_Q + (head * 5 * 64 + lane) * 16, epoch);
      s_q[lane] = pk(t[0], t[1]);
      lds_arrive(&s_cnt[4], lane);
      ST(4);
    }
    const int hp = head >> 1, tile = (head & 1) * 5 + sp;
    const int fin = wv != 1 ? -1 : sp == 5 ? head : sp == 6 && head < 8 ? 32 + head : -1;
    const int fcp = fin * 8 + c8;
    u32 gb = 0;
    if (fin >= 0) {
      const f32x2 mv = gather_sum<1, 0>(rw, WS_MOD + ((r8 & 3) * 1920 + 1600 + fcp) * 16, epoch);
      float b0 = lane < 32 ? mv[0] : 0.f, b1 = lane < 32 ? mv[1] : 0.f;
      b0 += dpp<DPP_R8>(b0); b0 = sw16(b0, b0); b0 = sw32(b0, b0);
      b1 += dpp<DPP_R8>(b1); b1 = sw16(b1, b1); b1 = sw32(b1, b1);
      gb = pk(b0, b1);
    }
    lds_wait(&s_cnt[3], nsw);
    {
      const rsrc_t ro = mkr(p.wo, qo_own ? 4096 * 640 * 2 : 0);
#pragma unroll
      for (int j = 0; j < 16; ++j) wb[j] = ld16<NT_W>(ro, ((hp * 2 + wv) * 128 + r8 * 16) * 1280 + tile * 128 + c8 * 16, j * 1280);
    }
    float fw[10], M, L;
    if (wv == 0) {
      lds_wait(&s_cnt[2], nsw);
      const f32x2 ml = ch < nsw ? s_ml[ch] : f32x2{-INFINITY, 0.f};
      M = ml[0];
      M = fmaxf(M, dpp<DPP_X1>(M)); M = fmaxf(M, dpp<DPP_X2>(M)); M = fmaxf(M, dpp<DPP_R4>(M)); M = fmaxf(M, dpp<DPP_R8>(M));
      const float f = __expf(ml[0] - M);
      L = row_sum(f * ml[1]);
      if (lane == 0) stg16(rw, WS_ML + (head * 8 + sp) * 64, M, L, epoch);
#pragma unroll
      for (int k = 0; k < 10; ++k) fw[k] = __int_as_float(__builtin_amdgcn_readlane(__float_as_int(f), k));
      ST(5);
    }
    FENCE();
    bar();
    if (wv == 0) {
      ST(6);
      f32x2 o = {0.f, 0.f};
#pragma unroll
      for (int k = 0; k < 9; ++k) o += fw[k] * *(f32x2*)&s_part[k][lane * 2];
      if (nsw == 10) o += fw[9] * *(f32x2*)&s_part[9][lane * 2];
      stg16(rw, WS_O + ((head * 7 + sp) * 64 + lane) * 16, o[0], o[1], epoch);
      ST(7);
    }
    if (qo_own) {
      u32x4 t0[8], t1[8], ov[8];
      transp<true>(wb, t0);
      transp<true>(wb + 8, t1);
      gather16<8, 1024>(rw, WS_O + ((hp * 2 + wv) * 7 * 64 + lane) * 16,
                        WS_ML + ((hp * 2 + wv) * 8 + (c8 < 7 ? c8 : 0)) * 64 - 7 * 1024, epoch, ov);
      if (wv == 0) ST(8);
      const float ms = __uint_as_float(ov[7][0]);
      float Mh = ms;
      Mh = fmaxf(Mh, dpp<DPP_X1>(Mh)); Mh = fmaxf(Mh, dpp<DPP_X2>(Mh)); Mh = fmaxf(Mh, dpp<DPP_R4>(Mh));
      const float es = __expf(ms - Mh);
      float Lh = c8 < 7 ? es * __uint_as_float(ov[7][2]) : 0.f;
      Lh += dpp<DPP_X1>(Lh); Lh += dpp<DPP_X2>(Lh); Lh += dpp<DPP_R4>(Lh);
      const float f = es * __builtin_amdgcn_rcpf(Lh);
      f32x2 t = {0.f, 0.f};
#pragma unroll
      for (int k = 0; k < 7; ++k)
        t += __int_as_float(__builtin_amdgcn_readlane(__float_as_int(f), k)) * f32x2{__uint_as_float(ov[k][0]), __uint_as_float(ov[k][2])};
      s_w[wv][lane] = pk(t[0], t[1]);
      FENCE();
      const u32x4 h0 = *(const u32x4*)&s_w[wv][r8 * 8], h1 = *(const u32x4*)&s_w[wv][r8 * 8 + 4];
      f32x4 acc[8] = ZERO84;
      mfma8(acc, t0, h0);
      mfma8(acc, t1, h1);
      if (lane < 8 || (lane >= 40 && lane < 48)) {
        float* d = &s_y[0][(wv * 2 + (lane >> 5)) * 64 + c8 * 8];
        *(f32x4*)d = f32x4{acc[0][0], acc[1][0], acc[2][0], acc[3][0]};
        *(f32x4*)(d + 4) = f32x4{acc[4][0], acc[5][0], acc[6][0], acc[7][0]};
      }
      bar();
      if (wv == 0) {
        const float* d = &s_y[0][(lane & 31) * 2];
        const f32x2 u = (*(f32x2*)d + *(f32x2*)(d + 64)) + (*(f32x2*)(d + 128) + *(f32x2*)(d + 192));
        if (lane < 32) stg8(rw, WS_Y + (hp * 320 + tile * 32 + lane) * 8, pk(u[0], u[1]), epoch);
        ST(9);
      }
    }
    if (fin >= 0) {
    const u32 x1 = gather8(rw, WS_X1 + fcp * 8, epoch);
    const u32 dq = WS_D + (r8 * 320 + fcp) * 16;
    u32x4 dv[7];
    gather16<7, 8 * 5120>(rw, dq, r8 < 6 ? dq : dq - 8 * 5120, epoch, dv);
    ST(14);
    float y0 = r8 < 6 ? __uint_as_float(dv[6][0]) : 0.f, y1 = r8 < 6 ? __uint_as_float(dv[6][2]) : 0.f;
#pragma unroll
    for (int k = 0; k < 6; ++k) { y0 += __uint_as_float(dv[k][0]); y1 += __uint_as_float(dv[k][2]); }
    y0 += dpp<DPP_R8>(y0); y0 = sw16(y0, y0); y0 = sw32(y0, y0);
    y1 += dpp<DPP_R8>(y1); y1 = sw16(y1, y1); y1 = sw32(y1, y1);
    if (lane < 8) ((u32*)p.out)[fcp] = resid(x1, gb, y0, y1);
    if (fin == 0 && lane == 0) __builtin_amdgcn_raw_buffer_store_b32(epoch, rw, WS_EPOCH, 0, 16);
    ST(15);
    }
  } else {
    PROLOGUE_A(wv < 2 && ex < 16, 224 + ex)
#define MLP_LOAD() {                                                                                                  \
      const char* src = (const char*)(lane & 8 ? p.wu : p.wg) + (wv * 64 + g * 16) * 3456 + ex * 128 + c8 * 16;      \
      _Pragma("unroll") for (int j = 0; j < 16; ++j)                                                                  \
        wb[j] = __builtin_nontemporal_load((const __attribute__((address_space(1))) u32x4*)(src + j * 3456)); }
    MLP_LOAD()
    const int cp = wv * 32 + ch * 2 + (g >> 1), hf = g & 1, cg = wv < 5 ? wv : wv - 5, rh = wv < 5 ? 0 : 1;
    const u32 xp = __builtin_amdgcn_raw_buffer_load_b32(rx, cp * 4, 0, 0);
    PROLOGUE_B()
    u32x4 wd[8];
    {
      const rsrc_t rd = mkr(p.wd, 1728 * 640 * 2);
#pragma unroll
      for (int j = 0; j < 8; ++j) wd[j] = ld16<NT_W>(rd, (ex * 64 + rh * 32 + g * 8) * 1280 + (cg * 16 + ch) * 16, j * 1280);
    }
    const f32x2 m0 = gather_sum<4, 1920 * 16>(rw, WS_MOD + (640 + hf * 320 + cp) * 16, epoch);
    const f32x2 m1 = gather_sum<4, 1920 * 16>(rw, WS_MOD + (1280 + cp) * 16, epoch);
    const u32 gs = pk(m0[0], m0[1]), cb = pk(m1[0], m1[1]);
    const auto gs2 = __builtin_amdgcn_permlane16_swap(gs, gs, false, false);
    u32x4 t0[8], t1[8], td[8];
    transp<true>(wb, t0);
    transp<true>(wb + 8, t1);
    transp<true>(wd, td);
    if (wv == 0) ST(10);
    const u32 yo = WS_Y + (g * 160 + wv * 16 + ch) * 16;
    u32x4 yv[4];
    gather16<4, 10240>(rw, yo, yo, epoch, yv);
    if (wv == 0) ST(11);
    float y0 = 0.f, y1 = 0.f, y2 = 0.f, y3 = 0.f;
#pragma unroll
    for (int k = 0; k < 4; ++k) { y0 += blo(yv[k][0]); y1 += bhi(yv[k][0]); y2 += blo(yv[k][2]); y3 += bhi(yv[k][2]); }
    y0 = sw32(y0, y2); y1 = sw32(y1, y3);
    const u32 x1 = resid(xp, gs2[0], sw16(y0, y0), sw16(y1, y1));
    if (ex == 0 && hf == 0) stg8(rw, WS_X1 + cp * 8, x1, epoch);
    {
      float a = row_sum(blo(x1) + bhi(x1));
      const float mw = sw32(a, a) * (1.f / 64.f), d0 = blo(x1) - mw, d1 = bhi(x1) - mw;
      a = row_sum(d0 * d0 + d1 * d1);
      a = sw32(a, a);
      if (lane == 0) s_ml[wv] = f32x2{mw, a};
    }
    bar();
    const f32x2 st = s_ml[ch < 10 ? ch : 0];
    const float mean = row_sum(ch < 10 ? st[0] : 0.f) * 0.1f, dm = st[0] - mean;
    const float var = row_sum(ch < 10 ? st[1] + 64.f * dm * dm : 0.f) * (1.f / 640.f);
    const float isd = __builtin_amdgcn_rcpf(rb(__builtin_amdgcn_sqrtf(rb(rb(var) + 1e-5f))));
    s_w[wv][hf * 32 + ch * 2 + (g >> 1)] = modulate(x1, rb(mean), isd, cb, gs2[1]);
    FENCE();
    {
      const u32x4 h0 = *(const u32x4*)&s_w[wv][g * 8], h1 = *(const u32x4*)&s_w[wv][g * 8 + 4];
      f32x4 acc[8] = ZERO84;
      mfma8(acc, t0, h0);
      mfma8(acc, t1, h1);
      *(f32x2*)&s_part[wv][ch * 8 + g * 2] = pick2(acc, g);
    }
    if (wv == 0) ST(12);
    bar();
    float ga = 0.f, ua = 0.f, gb = 0.f, ub = 0.f;
#pragma unroll
    for (int k = 0; k < 5; ++k) {
      ga += s_part[k][lane]; ua += s_part[k][64 + lane]; gb += s_part[5 + k][lane]; ub += s_part[5 + k][64 + lane];
    }
    const float hh = rb(gelu_tanh(rb(ga + gb))) * rb(ua + ub);
    const u32 hpair = pk(hh, dpp<DPP_X1>(hh));
    if (!(lane & 1)) s_w[wv][lane >> 1] = hpair;
    FENCE();
    f32x4 acc[8] = ZERO84;
    mfma8(acc, td, *(const u32x4*)&s_w[wv][rh * 16 + g * 4]);
    const f32x2 t = pick2(acc, g);
    stg16(rw, WS_D + ((ex * 2 + rh) * 320 + (cg * 16 + ch) * 4 + g) * 16, t[0], t[1], epoch);
    if (wv == 0) ST(13);
  }
}

extern "C" void kernel_launch(void* const* d_in, const int* in_sizes, int n_in, void* d_out, int out_size,
                              void* d_ws, size_t ws_size, hipStream_t stream) {
  Params p;
  p.x = (const unsigned short*)d_in[0];  p.k = (const unsigned short*)d_in[1];
  p.v = (const unsigned short*)d_in[2];  p.wq = (const unsigned short*)d_in[3];
  p.wo = (const unsigned short*)d_in[4]; p.wg = (const unsigned short*)d_in[6];
  p.wu = (const unsigned short*)d_in[7]; p.wd = (const unsigned short*)d_in[8];
  p.c = (const unsigned short*)d_in[9];  p.wm = (const unsigned short*)d_in[10];
  p.out = (unsigned short*)d_out;        p.ws = (char*)d_ws;
  fused_block<<<256, 768, 0, stream>>>(p);
}
```

```cpp
#include <hip/hip_runtime.h>
#include <hip/hip_bf16.h>

typedef unsigned u32;
typedef unsigned long long u64;
typedef u32 u32x4 __attribute__((ext_vector_type(4)));
typedef u32 u32x2 __attribute__((ext_vector_type(2)));
typedef __bf16 bf16x2 __attribute__((ext_vector_type(2)));
typedef float f32x2 __attribute__((ext_vector_type(2)));
typedef __amdgpu_buffer_rsrc_t rsrc_t;

#define KWIN 3
#define KW0 3
#define NS 8
#ifndef NV0
#define NV0 4
#endif
#ifndef NV1
#define NV1 16
#endif
#define NT_KV 2
#define NT_W 2
#define CODE_BYTES 18432u
#define GSLEEP 0
#define SPIN_CAP 400000u

#define WS_EPOCH 0
#define WS_X1    1024
#define WS_ML    8192
#define WS_MOD   (WS_ML + 256 * 64)
#define WS_Y     (WS_MOD + 4 * 3840 * 8)
#define WS_D     (WS_Y + 16 * 320 * 8)
#define WS_Q     (WS_D + 54 * 640 * 8)
#define WS_O     (WS_Q + 32 * 5 * 128 * 8)
#define WS_END   (WS_O + 32 * 7 * 128 * 8)

#define FENCE() asm volatile("" ::: "memory")

struct Params {
  const unsigned short *x, *k, *v, *wq, *wo, *wg, *wu, *wd, *c, *wm;
  unsigned short* out;
  char* ws;
};

__device__ __forceinline__ float blo(u32 a) { return __uint_as_float(a << 16); }
__device__ __forceinline__ float bhi(u32 a) { return __uint_as_float(a & 0xffff0000u); }
__device__ __forceinline__ u32 pk(float a, float b) {
  bf16x2 r = {(__bf16)a, (__bf16)b};
  return __builtin_bit_cast(u32, r);
}
__device__ __forceinline__ float rb(float a) { return (float)(__bf16)a; }
__device__ __forceinline__ float dot2(u32 a, u32 b, float c) {
  return __builtin_amdgcn_fdot2_f32_bf16(__builtin_bit_cast(bf16x2, a), __builtin_bit_cast(bf16x2, b), c, false);
}
template <int C> __device__ __forceinline__ float dpp(float a) {
  return __int_as_float(__builtin_amdgcn_update_dpp(0, __float_as_int(a), C, 0xf, 0xf, true));
}
#define DPP_X1 0xB1
#define DPP_X2 0x4E
#define DPP_R4 0x124
#define DPP_R8 0x128
__device__ __forceinline__ float sw32(float a, float b) {
  auto r = __builtin_amdgcn_permlane32_swap(__float_as_uint(a), __float_as_uint(b), false, false);
  return __uint_as_float(r[0]) + __uint_as_float(r[1]);
}
__device__ __forceinline__ float sw16(float a, float b) {
  auto r = __builtin_amdgcn_permlane16_swap(__float_as_uint(a), __float_as_uint(b), false, false);
  return __uint_as_float(r[0]) + __uint_as_float(r[1]);
}
__device__ __forceinline__ float mx32(float a) {
  auto r = __builtin_amdgcn_permlane32_swap(__float_as_uint(a), __float_as_uint(a), false, false);
  return fmaxf(__uint_as_float(r[0]), __uint_as_float(r[1]));
}
__device__ __forceinline__ float mx16(float a) {
  auto r = __builtin_amdgcn_permlane16_swap(__float_as_uint(a), __float_as_uint(a), false, false);
  return fmaxf(__uint_as_float(r[0]), __uint_as_float(r[1]));
}
__device__ __forceinline__ float row_sum(float a) {
  a += dpp<DPP_X1>(a); a += dpp<DPP_X2>(a); a += dpp<DPP_R4>(a); a += dpp<DPP_R8>(a); return a;
}
__device__ __forceinline__ float wave_sum(float a) { a = row_sum(a); a = sw16(a, a); return sw32(a, a); }
__device__ __forceinline__ void reduce8(const float (&a)[8], float& r0, float& r1) {
  float b0 = sw32(a[0], a[4]), b1 = sw32(a[1], a[5]), b2 = sw32(a[2], a[6]), b3 = sw32(a[3], a[7]);
  r0 = sw16(b0, b2); r1 = sw16(b1, b3);
}
__device__ __forceinline__ void mac8(float (&acc)[8], u32x4 w, float a) {
#pragma unroll
  for (int j = 0; j < 4; ++j) { acc[2 * j] += a * blo(w[j]); acc[2 * j + 1] += a * bhi(w[j]); }
}
__device__ __forceinline__ void mac8x2(float (&acc)[8], u32x4 w0, u32x4 w1, u32 pair) {
#pragma unroll
  for (int j = 0; j < 4; ++j) {
    acc[2 * j]     = dot2(__builtin_amdgcn_perm(w1[j], w0[j], 0x05040100u), pair, acc[2 * j]);
    acc[2 * j + 1] = dot2(__builtin_amdgcn_perm(w1[j], w0[j], 0x07060302u), pair, acc[2 * j + 1]);
  }
}
__device__ __forceinline__ rsrc_t mkr(const void* ptr, u32 bytes) {
  return __builtin_amdgcn_make_buffer_rsrc((void*)ptr, 0, bytes, 0x00020000);
}
template <int AUX> __device__ __forceinline__ u32x4 ld16(rsrc_t r, u32 off, u32 soff = 0) {
  return __builtin_amdgcn_raw_buffer_load_b128(r, off, soff, AUX);
}
__device__ __forceinline__ u32x2 ldg8(rsrc_t r, u32 off) { return __builtin_amdgcn_raw_buffer_load_b64(r, off, 0, 16); }
__device__ __forceinline__ void stg16(rsrc_t r, u32 off, float a, float b, u32 e) {
  u32x4 v = {__float_as_uint(a), e, __float_as_uint(b), e};
  __builtin_amdgcn_raw_buffer_store_b128(v, r, off, 0, 16);
}
__device__ __forceinline__ void stg8(rsrc_t r, u32 off, u32 a, u32 e) {
  u32x2 v = {a, e};
  __builtin_amdgcn_raw_buffer_store_b64(v, r, off, 0, 16);
}
template <int N, int S> __device__ __forceinline__ void gather16(rsrc_t r, u32 off, u32 offl, u32 e, u32x4 (&v)[N]) {
#pragma nounroll
  for (u32 spins = 0; spins < SPIN_CAP; ++spins) {
    FENCE();
    u32 bad = 0;
#pragma unroll
    for (int k = 0; k < N; ++k) v[k] = k < N - 1 ? ld16<16>(r, off, k * S) : ld16<16>(r, offl, k * S);
#pragma unroll
    for (int k = 0; k < N; ++k) bad |= (v[k][1] ^ e) | (v[k][3] ^ e);
    if (__all(bad == 0)) break;
    __builtin_amdgcn_s_sleep(GSLEEP);
  }
  FENCE();
}
__device__ __forceinline__ u32 gather8(rsrc_t r, u32 off, u32 e) {
  u32x2 t;
#pragma nounroll
  for (u32 spins = 0; spins < SPIN_CAP; ++spins) {
    FENCE();
    t = ldg8(r, off);
    if (__all(t[1] == e)) break;
    __builtin_amdgcn_s_sleep(1);
  }
  FENCE();
  return t[0];
}
template <int N, int S> __device__ __forceinline__ f32x2 gather_sum(rsrc_t r, u32 off, u32 e) {
  u32x4 v[N];
  gather16<N, S>(r, off, off, e, v);
  f32x2 t = {__uint_as_float(v[0][0]), __uint_as_float(v[0][2])};
#pragma unroll
  for (int k = 1; k < N; ++k) t += f32x2{__uint_as_float(v[k][0]), __uint_as_float(v[k][2])};
  return t;
}
__device__ __forceinline__ void bar() {
  asm volatile("s_waitcnt lgkmcnt(0)\n\ts_barrier" ::: "memory");
}
__device__ __forceinline__ void lds_arrive(u32* cnt, int lane) {
  asm volatile("s_waitcnt lgkmcnt(0)" ::: "memory");
  if (lane == 0) __hip_atomic_fetch_add(cnt, 1u, __ATOMIC_RELAXED, __HIP_MEMORY_SCOPE_WORKGROUP);
}
__device__ __forceinline__ void lds_wait(u32* cnt, u32 n) {
  while (__hip_atomic_load(cnt, __ATOMIC_RELAXED, __HIP_MEMORY_SCOPE_WORKGROUP) < n) __builtin_amdgcn_s_sleep(1);
  FENCE();
}
__device__ __forceinline__ void ln_stats(const u32 (&x)[5], float& m, float& isd) {
  float s = 0.f;
#pragma unroll
  for (int i = 0; i < 5; ++i) s += blo(x[i]) + bhi(x[i]);
  const float mean = wave_sum(s) * (1.f / 640.f);
  float q = 0.f;
#pragma unroll
  for (int i = 0; i < 5; ++i) { const float d0 = blo(x[i]) - mean, d1 = bhi(x[i]) - mean; q += d0 * d0 + d1 * d1; }
  const float var = wave_sum(q) * (1.f / 640.f);
  m = rb(mean);
  isd = __builtin_amdgcn_rcpf(rb(__builtin_amdgcn_sqrtf(rb(rb(var) + 1e-5f))));
}
__device__ __forceinline__ u32 modulate(u32 x, float m, float isd, u32 sc, u32 sh) {
  return pk(rb(rb(rb(blo(x) - m) * isd) * rb(1.f + blo(sc))) + blo(sh),
            rb(rb(rb(bhi(x) - m) * isd) * rb(1.f + bhi(sc))) + bhi(sh));
}
__device__ __forceinline__ u32 resid(u32 x, u32 gate, float y0, float y1) {
  return pk(blo(x) + rb(blo(gate) * rb(y0)), bhi(x) + rb(bhi(gate) * rb(y1)));
}
typedef __bf16 bf16x8 __attribute__((ext_vector_type(8)));
typedef float f32x4 __attribute__((ext_vector_type(4)));
template <bool PIN> __device__ __forceinline__ void transp(const u32x4* w, u32x4 (&t)[8]) {
#pragma unroll
  for (int j = 0; j < 4; ++j)
#pragma unroll
    for (int k = 0; k < 4; ++k) {
      t[2 * j][k] = __builtin_amdgcn_perm(w[2 * k + 1][j], w[2 * k][j], 0x05040100u);
      t[2 * j + 1][k] = __builtin_amdgcn_perm(w[2 * k + 1][j], w[2 * k][j], 0x07060302u);
    }
  if (PIN) {
    asm volatile("" : "+v"(t[0]), "+v"(t[1]), "+v"(t[2]), "+v"(t[3]), "+v"(t[4]), "+v"(t[5]), "+v"(t[6]), "+v"(t[7]));
    __builtin_amdgcn_sched_barrier(0);
  }
}
__device__ __forceinline__ void mfma8(f32x4 (&acc)[8], const u32x4 (&t)[8], u32x4 a) {
#pragma unroll
  for (int e = 0; e < 8; ++e)
    acc[e] = __builtin_amdgcn_mfma_f32_16x16x32_bf16(__builtin_bit_cast(bf16x8, a), __builtin_bit_cast(bf16x8, t[e]), acc[e], 0, 0, 0);
}
__device__ __forceinline__ f32x2 pick2(const f32x4 (&a)[8], int g) {
  const bool o = g & 1, h = g & 2;
  float a0 = a[0][0], a1 = a[1][0], a2 = a[2][0], a3 = a[3][0], a4 = a[4][0], a5 = a[5][0], a6 = a[6][0], a7 = a[7][0];
  asm volatile("" : "+v"(a0), "+v"(a1), "+v"(a2), "+v"(a3), "+v"(a4), "+v"(a5), "+v"(a6), "+v"(a7));
  return f32x2{h ? (o ? a6 : a4) : (o ? a2 : a0), h ? (o ? a7 : a5) : (o ? a3 : a1)};
}
#define Z4 f32x4{0.f, 0.f, 0.f, 0.f}
#define ZERO84 {Z4, Z4, Z4, Z4, Z4, Z4, Z4, Z4}
#define ZERO8 {0.f, 0.f, 0.f, 0.f, 0.f, 0.f, 0.f, 0.f}
__device__ __forceinline__ float gelu_tanh(float g) {
  const float z = 0.7978845608028654f * (g + 0.044715f * g * g * g);
  const float t = 1.f - 2.f * __builtin_amdgcn_rcpf(__expf(2.f * z) + 1.f);
  return 0.5f * g * (1.f + t);
}

__device__ __forceinline__ void mod_issue(const Params& p, bool own, int unit, int tid, u32x4 (&wa)[10], u32 (&craw)[10]) {
  const rsrc_t rm = mkr(p.wm, own ? 640 * 3840 * 2 : 0), rc = mkr(p.c, own ? 1280 : 0);
  const int row0 = (unit & 3) * 160 + (tid >> 3);
#pragma unroll
  for (int j = 0; j < 10; ++j) wa[j] = ld16<NT_W>(rm, row0 * 7680 + (unit >> 2) * 128 + (tid & 7) * 16, j * 16 * 7680);
#pragma unroll
  for (int j = 0; j < 10; ++j) craw[j] = __builtin_amdgcn_raw_buffer_load_b16(rc, row0 * 2, j * 32, 0);
}
__device__ __forceinline__ void mod_finish(rsrc_t rw, int unit, int wv, int lane, u32 epoch, const u32x4 (&wa)[10],
                                           const u32 (&craw)[10], float* s_t, u32* cnt) {
  float acc[8] = ZERO8;
#pragma unroll
  for (int j = 0; j < 10; ++j) {
    const float cv = blo(craw[j]);
    mac8(acc, wa[j], rb(cv * __builtin_amdgcn_rcpf(1.f + __expf(-cv))));
  }
  float a0, a1;
  reduce8(acc, a0, a1);
  a0 += dpp<DPP_R8>(a0); a1 += dpp<DPP_R8>(a1);
  if (wv == 1) { *(f32x2*)&s_t[lane * 2] = f32x2{a0, a1}; lds_arrive(cnt, lane); }
  else {
    lds_wait(cnt, 1);
    const f32x2 t = *(f32x2*)&s_t[lane * 2];
    if (!(lane & 8))
      stg16(rw, WS_MOD + ((unit & 3) * 3840 + (unit >> 2) * 64 + (lane & 7) * 8 + (lane >> 4) * 2) * 8, a0 + t[0], a1 + t[1], epoch);
  }
}

__global__ void __launch_bounds__(768, 3) fused_block(Params p) {
  __shared__ __attribute__((aligned(16))) float s_part[10][128];
  __shared__ __attribute__((aligned(16))) float s_y[2][256];
  __shared__ __attribute__((aligned(16))) u32 s_w[10][64];
  __shared__ __attribute__((aligned(16))) u32 s_q[64];
  __shared__ __attribute__((aligned(16))) f32x2 s_ml[10];
  __shared__ u32 s_cnt[5];
  const int tid = threadIdx.x, lane = tid & 63, g = lane >> 4, ch = lane & 15, c8 = lane & 7, r8 = lane >> 3;
  const int wv = __builtin_amdgcn_readfirstlane(tid >> 6);
  const int b = blockIdx.x, xcd = b & 7, r = b >> 3;
  const bool attn = r < 28;
  const int head = xcd * 4 + (r & 3), sp = r >> 2;
  const int ex = (r - 28) * 8 + xcd;
  const int nsw = sp == 6 ? 10 : 9;
  if (r == 0 && wv == 11) {
    u64 pc;
    asm volatile("s_getpc_b64 %0" : "=s"(pc));
    const rsrc_t rc = mkr((const void*)pc, CODE_BYTES);
    u32 t = 0;
#pragma unroll
    for (int j = 0; j < 3; ++j) t |= __builtin_amdgcn_raw_buffer_load_b32(rc, lane * 128 + j * 8192, 0, 0);
    asm volatile("" :: "v"(t));
  }
  if (wv >= (attn ? 2 + nsw : ex < 27 ? 10 : 0)) return;

  if (attn && wv >= 2) {
    const int w = wv - 2;
    const size_t base = ((size_t)head * 4096 + (sp * 9 + w) * 64) * 128;
    const rsrc_t rk = mkr(p.k + base, 16384), rv = mkr(p.v + base, 16384);
    u32x4 K[16], V[16];
#define LDV(i) V[i] = ld16<NT_KV>(rv, lane * 16, (i) * 1024)
#pragma unroll
    for (int i = 0; i < 16 + NV0; ++i) {
      if (i == KW0) bar();
      if (i >= KW0 && i < NS) asm volatile("s_waitcnt vmcnt(%0)" :: "n"(KW0 - 1) : "memory");
      if (i >= KW0 && i >= NS) asm volatile("s_waitcnt vmcnt(%0)" :: "n"(KWIN - 1) : "memory");
      if (i == 0) LDV(0);
      else if (i <= 16) K[i - 1] = ld16<NT_KV>(rk, lane * 16, (i - 1) * 1024);
      else LDV(i - 16);
      FENCE();
    }
    lds_wait(&s_cnt[4], 1);
#pragma unroll
    for (int i = NV0; i < NV1; ++i) LDV(i);
    FENCE();
    lds_arrive(&s_cnt[3], lane);
    const u32x4 q = *(const u32x4*)&s_q[ch * 4];
    float sa[16];
#pragma unroll
    for (int i = 0; i < 16; ++i)
      sa[i] = row_sum(dot2(K[i][3], q[3], dot2(K[i][2], q[2], dot2(K[i][1], q[1], dot2(K[i][0], q[0], 0.f)))));
    float sc[16], m = -INFINITY;
#pragma unroll
    for (int i = 0; i < 8; ++i) {
      const u32 a = pk(sa[2 * i], sa[2 * i + 1]);
      const u32 c = pk(blo(a) * 0.08838834764831845f, bhi(a) * 0.08838834764831845f);
      sc[2 * i] = blo(c); sc[2 * i + 1] = bhi(c);
      m = fmaxf(m, fmaxf(sc[2 * i], sc[2 * i + 1]));
    }
    m = mx32(mx16(m));
    float l = 0.f;
    u32 pp[8];
#pragma unroll
    for (int i = 0; i < 8; ++i) {
      const float e0 = __expf(sc[2 * i] - m), e1 = __expf(sc[2 * i + 1] - m);
      l += e0 + e1;
      pp[i] = pk(e0, e1);
    }
    l = sw16(l, l); l = sw32(l, l);
    if (lane == 0) s_ml[w] = f32x2{m, l};
    lds_arrive(&s_cnt[2], lane);
#pragma unroll
    for (int i = NV1; i < 16; ++i) LDV(i);
    FENCE();
    f32x4 acc[8] = ZERO84;
    u32x4 t[8];
    transp<false>(V, t);
    mfma8(acc, t, u32x4{pp[0], pp[1], pp[2], pp[3]});
    transp<false>(V + 8, t);
    mfma8(acc, t, u32x4{pp[4], pp[5], pp[6], pp[7]});
    *(f32x2*)&s_part[w][ch * 8 + g * 2] = pick2(acc, g);
    bar();
    return;
  }

  const rsrc_t rw = mkr(p.ws, WS_END), rx = mkr(p.x, 1280);
  __builtin_amdgcn_s_setprio(2);
  const bool qo_own = attn && sp < 5;
#define PROLOGUE_A(MOD_OWN, UNIT)                                                                                     \
  const bool mod_own = (MOD_OWN);                                                                                     \
  const int unit = (UNIT);                                                                                            \
  u32x4 wa[10], wb[16];                                                                                               \
  u32 craw[10], xraw[5];                                                                                              \
  mod_issue(p, mod_own, unit, tid, wa, craw);                                                                         \
  const u32 eraw = __builtin_amdgcn_raw_buffer_load_b32(rw, WS_EPOCH, 0, 16);                                         \
  _Pragma("unroll") for (int j = 0; j < 5; ++j) xraw[j] = __builtin_amdgcn_raw_buffer_load_b32(rx, lane * 4, j * 256, 0);
#define PROLOGUE_B()                                                                                                  \
  if (tid == 0) { s_cnt[0] = 0u; s_cnt[1] = 0u; s_cnt[2] = 0u; s_cnt[3] = 0u; s_cnt[4] = 0u; }                                                \
  __builtin_amdgcn_sched_barrier(0);                                                                                  \
  bar();                                                                                                       \
  __builtin_amdgcn_sched_barrier(0);                                                                                  \
  const u32 epoch = __builtin_amdgcn_readfirstlane(eraw) + 1u;                                                        \
  if (mod_own) mod_finish(rw, unit, wv, lane, epoch, wa, craw, &s_part[0][0], &s_cnt[0]);

  if (attn) {
    PROLOGUE_A(true, sp >= 5 ? head * 2 + sp - 5 : sp == 4 ? (head < 16 ? 64 : 192) + head : 80 + head * 4 + sp)
    const u32 xq = __builtin_amdgcn_raw_buffer_load_b32(rx, ((qo_own ? sp * 64 + wv * 32 : 0) + (lane & 31)) * 4, 0, 0);
    {
      const rsrc_t rq = mkr(p.wq, qo_own ? 640 * 4096 * 2 : 0);
#pragma unroll
      for (int j = 0; j < 16; ++j) wb[j] = ld16<NT_W>(rq, (sp * 128 + wv * 64 + g * 16) * 8192 + head * 256 + ch * 16, j * 8192);
    }
    const u32 touch = __builtin_amdgcn_raw_buffer_load_b32(mkr(p.wo, qo_own ? 4096 * 640 * 2 : 0),
                                                           (((head >> 1) * 2 + wv) * 128 + (lane & 3) * 32) * 1280, 0, 0);
    PROLOGUE_B()
    asm volatile("" :: "v"(touch));
    if (qo_own) {
      float xm, xi;
      ln_stats(xraw, xm, xi);
      const f32x2 t = gather_sum<4, 1920 * 16>(rw, WS_MOD + ((lane >> 5) * 320 + sp * 64 + wv * 32 + (lane & 31)) * 16, epoch);
      const u32 mp = pk(t[0], t[1]);
      const auto sc2 = __builtin_amdgcn_permlane32_swap(mp, mp, false, false);
      s_w[wv][lane] = modulate(xq, xm, xi, sc2[1], sc2[0]);
      FENCE();
      const u32x4 h0 = *(const u32x4*)&s_w[wv][g * 8], h1 = *(const u32x4*)&s_w[wv][g * 8 + 4];
      float acc[8] = ZERO8;
#pragma unroll
      for (int j = 0; j < 8; ++j) mac8x2(acc, wb[2 * j], wb[2 * j + 1], j < 4 ? h0[j & 3] : h1[j & 3]);
      float a0, a1;
      reduce8(acc, a0, a1);
      if (wv == 1) { *(f32x2*)&s_y[1][lane * 2] = f32x2{a0, a1}; lds_arrive(&s_cnt[1], lane); }
      else {
        lds_wait(&s_cnt[1], 1);
        const f32x2 u = *(f32x2*)&s_y[1][lane * 2];
        stg16(rw, WS_Q + ((head * 5 + sp) * 64 + ch * 4 + g) * 16, a0 + u[0], a1 + u[1], epoch);
      }
    }
    if (wv == 0) {
      const f32x2 t = gather_sum<5, 1024>(rw, WS_Q + (head * 5 * 64 + lane) * 16, epoch);
      s_q[lane] = pk(t[0], t[1]);
      lds_arrive(&s_cnt[4], lane);
    }
    const int hp = head >> 1, tile = (head & 1) * 5 + sp;
    const int fin = wv != 1 ? -1 : sp == 5 ? head : sp == 6 && head < 8 ? 32 + head : -1;
    const int fcp = fin * 8 + c8;
    u32 gb = 0;
    if (fin >= 0) {
      if (lane < 8) ((u32*)p.out)[fcp] = 0u;
      const f32x2 mv = gather_sum<1, 0>(rw, WS_MOD + ((r8 & 3) * 1920 + 1600 + fcp) * 16, epoch);
      float b0 = lane < 32 ? mv[0] : 0.f, b1 = lane < 32 ? mv[1] : 0.f;
      b0 += dpp<DPP_R8>(b0); b0 = sw16(b0, b0); b0 = sw32(b0, b0);
      b1 += dpp<DPP_R8>(b1); b1 = sw16(b1, b1); b1 = sw32(b1, b1);
      gb = pk(b0, b1);
    }
    lds_wait(&s_cnt[3], nsw);
    {
      const rsrc_t ro = mkr(p.wo, qo_own ? 4096 * 640 * 2 : 0);
#pragma unroll
      for (int j = 0; j < 16; ++j) wb[j] = ld16<NT_W>(ro, ((hp * 2 + wv) * 128 + r8 * 16) * 1280 + tile * 128 + c8 * 16, j * 1280);
    }
    float fw[10], M, L;
    if (wv == 0) {
      lds_wait(&s_cnt[2], nsw);
      const f32x2 ml = ch < nsw ? s_ml[ch] : f32x2{-INFINITY, 0.f};
      M = ml[0];
      M = fmaxf(M, dpp<DPP_X1>(M)); M = fmaxf(M, dpp<DPP_X2>(M)); M = fmaxf(M, dpp<DPP_R4>(M)); M = fmaxf(M, dpp<DPP_R8>(M));
      const float f = __expf(ml[0] - M);
      L = row_sum(f * ml[1]);
      if (lane == 0) stg16(rw, WS_ML + (head * 8 + sp) * 64, M, L, epoch);
#pragma unroll
      for (int k = 0; k < 10; ++k) fw[k] = __int_as_float(__builtin_amdgcn_readlane(__float_as_int(f), k));
    }
    FENCE();
    bar();
    if (wv == 0) {
      f32x2 o = {0.f, 0.f};
#pragma unroll
      for (int k = 0; k < 9; ++k) o += fw[k] * *(f32x2*)&s_part[k][lane * 2];
      if (nsw == 10) o += fw[9] * *(f32x2*)&s_part[9][lane * 2];
      stg16(rw, WS_O + ((head * 7 + sp) * 64 + lane) * 16, o[0], o[1], epoch);
    }
    if (qo_own) {
      u32x4 t0[8], t1[8], ov[8];
      transp<true>(wb, t0);
      transp<true>(wb + 8, t1);
      gather16<8, 1024>(rw, WS_O + ((hp * 2 + wv) * 7 * 64 + lane) * 16,
                        WS_ML + ((hp * 2 + wv) * 8 + (c8 < 7 ? c8 : 0)) * 64 - 7 * 1024, epoch, ov);
      const float ms = __uint_as_float(ov[7][0]);
      float Mh = ms;
      Mh = fmaxf(Mh, dpp<DPP_X1>(Mh)); Mh = fmaxf(Mh, dpp<DPP_X2>(Mh)); Mh = fmaxf(Mh, dpp<DPP_R4>(Mh));
      const float es = __expf(ms - Mh);
      float Lh = c8 < 7 ? es * __uint_as_float(ov[7][2]) : 0.f;
      Lh += dpp<DPP_X1>(Lh); Lh += dpp<DPP_X2>(Lh); Lh += dpp<DPP_R4>(Lh);
      const float f = es * __builtin_amdgcn_rcpf(Lh);
      f32x2 t = {0.f, 0.f};
#pragma unroll
      for (int k = 0; k < 7; ++k)
        t += __int_as_float(__builtin_amdgcn_readlane(__float_as_int(f), k)) * f32x2{__uint_as_float(ov[k][0]), __uint_as_float(ov[k][2])};
      s_w[wv][lane] = pk(t[0], t[1]);
      FENCE();
      const u32x4 h0 = *(const u32x4*)&s_w[wv][r8 * 8], h1 = *(const u32x4*)&s_w[wv][r8 * 8 + 4];
      f32x4 acc[8] = ZERO84;
      mfma8(acc, t0, h0);
      mfma8(acc, t1, h1);
      if (lane < 8 || (lane >= 40 && lane < 48)) {
        float* d = &s_y[0][(wv * 2 + (lane >> 5)) * 64 + c8 * 8];
        *(f32x4*)d = f32x4{acc[0][0], acc[1][0], acc[2][0], acc[3][0]};
        *(f32x4*)(d + 4) = f32x4{acc[4][0], acc[5][0], acc[6][0], acc[7][0]};
      }
      bar();
      if (wv == 0) {
        const float* d = &s_y[0][(lane & 31) * 2];
        const f32x2 u = (*(f32x2*)d + *(f32x2*)(d + 64)) + (*(f32x2*)(d + 128) + *(f32x2*)(d + 192));
        if (lane < 32) stg8(rw, WS_Y + (hp * 320 + tile * 32 + lane) * 8, pk(u[0], u[1]), epoch);
      }
    }
    if (fin >= 0) {
    const u32 x1 = gather8(rw, WS_X1 + fcp * 8, epoch);
    const u32 dq = WS_D + (r8 * 320 + fcp) * 16;
    u32x4 dv[7];
    gather16<7, 8 * 5120>(rw, dq, r8 < 6 ? dq : dq - 8 * 5120, epoch, dv);
    float y0 = r8 < 6 ? __uint_as_float(dv[6][0]) : 0.f, y1 = r8 < 6 ? __uint_as_float(dv[6][2]) : 0.f;
#pragma unroll
    for (int k = 0; k < 6; ++k) { y0 += __uint_as_float(dv[k][0]); y1 += __uint_as_float(dv[k][2]); }
    y0 += dpp<DPP_R8>(y0); y0 = sw16(y0, y0); y0 = sw32(y0, y0);
    y1 += dpp<DPP_R8>(y1); y1 = sw16(y1, y1); y1 = sw32(y1, y1);
    if (lane < 8) ((u32*)p.out)[fcp] = resid(x1, gb, y0, y1);
    if (fin == 0 && lane == 0) __builtin_amdgcn_raw_buffer_store_b32(epoch, rw, WS_EPOCH, 0, 16);
    }
  } else {
    PROLOGUE_A(wv < 2 && ex < 16, 224 + ex)
#define MLP_LOAD() {                                                                                                  \
      const char* src = (const char*)(lane & 8 ? p.wu : p.wg) + (wv * 64 + g * 16) * 3456 + ex * 128 + c8 * 16;      \
      _Pragma("unroll") for (int j = 0; j < 16; ++j)                                                                  \
        wb[j] = __builtin_nontemporal_load((const __attribute__((address_space(1))) u32x4*)(src + j * 3456)); }
    MLP_LOAD()
    const int cp = wv * 32 + ch * 2 + (g >> 1), hf = g & 1, cg = wv < 5 ? wv : wv - 5, rh = wv < 5 ? 0 : 1;
    const u32 xp = __builtin_amdgcn_raw_buffer_load_b32(rx, cp * 4, 0, 0);
    PROLOGUE_B()
    u32x4 wd[8];
    {
      const rsrc_t rd = mkr(p.wd, 1728 * 640 * 2);
#pragma unroll
      for (int j = 0; j < 8; ++j) wd[j] = ld16<NT_W>(rd, (ex * 64 + rh * 32 + g * 8) * 1280 + (cg * 16 + ch) * 16, j * 1280);
    }
    const f32x2 m0 = gather_sum<4, 1920 * 16>(rw, WS_MOD + (640 + hf * 320 + cp) * 16, epoch);
    const f32x2 m1 = gather_sum<4, 1920 * 16>(rw, WS_MOD + (1280 + cp) * 16, epoch);
    const u32 gs = pk(m0[0], m0[1]), cb = pk(m1[0], m1[1]);
    const auto gs2 = __builtin_amdgcn_permlane16_swap(gs, gs, false, false);
    u32x4 t0[8], t1[8], td[8];
    transp<true>(wb, t0);
    transp<true>(wb + 8, t1);
    transp<true>(wd, td);
    const u32 yo = WS_Y + (g * 160 + wv * 16 + ch) * 16;
    u32x4 yv[4];
    gather16<4, 10240>(rw, yo, yo, epoch, yv);
    float y0 = 0.f, y1 = 0.f, y2 = 0.f, y3 = 0.f;
#pragma unroll
    for (int k = 0; k < 4; ++k) { y0 += blo(yv[k][0]); y1 += bhi(yv[k][0]); y2 += blo(yv[k][2]); y3 += bhi(yv[k][2]); }
    y0 = sw32(y0, y2); y1 = sw32(y1, y3);
    const u32 x1 = resid(xp, gs2[0], sw16(y0, y0), sw16(y1, y1));
    if (ex == 0 && hf == 0) stg8(rw, WS_X1 + cp * 8, x1, epoch);
    {
      float a = row_sum(blo(x1) + bhi(x1));
      const float mw = sw32(a, a) * (1.f / 64.f), d0 = blo(x1) - mw, d1 = bhi(x1) - mw;
      a = row_sum(d0 * d0 + d1 * d1);
      a = sw32(a, a);
      if (lane == 0) s_ml[wv] = f32x2{mw, a};
    }
    bar();
    const f32x2 st = s_ml[ch < 10 ? ch : 0];
    const float mean = row_sum(ch < 10 ? st[0] : 0.f) * 0.1f, dm = st[0] - mean;
    const float var = row_sum(ch < 10 ? st[1] + 64.f * dm * dm : 0.f) * (1.f / 640.f);
    const float isd = __builtin_amdgcn_rcpf(rb(__builtin_amdgcn_sqrtf(rb(rb(var) + 1e-5f))));
    s_w[wv][hf * 32 + ch * 2 + (g >> 1)] = modulate(x1, rb(mean), isd, cb, gs2[1]);
    FENCE();
    {
      const u32x4 h0 = *(const u32x4*)&s_w[wv][g * 8], h1 = *(const u32x4*)&s_w[wv][g * 8 + 4];
      f32x4 acc[8] = ZERO84;
      mfma8(acc, t0, h0);
      mfma8(acc, t1, h1);
      *(f32x2*)&s_part[wv][ch * 8 + g * 2] = pick2(acc, g);
    }
    bar();
    float ga = 0.f, ua = 0.f, gb = 0.f, ub = 0.f;
#pragma unroll
    for (int k = 0; k < 5; ++k) {
      ga += s_part[k][lane]; ua += s_part[k][64 + lane]; gb += s_part[5 + k][lane]; ub += s_part[5 + k][64 + lane];
    }
    const float hh = rb(gelu_tanh(rb(ga + gb))) * rb(ua + ub);
    const u32 hpair = pk(hh, dpp<DPP_X1>(hh));
    if (!(lane & 1)) s_w[wv][lane >> 1] = hpair;
    FENCE();
    f32x4 acc[8] = ZERO84;
    mfma8(acc, td, *(const u32x4*)&s_w[wv][rh * 16 + g * 4]);
    const f32x2 t = pick2(acc, g);
    stg16(rw, WS_D + ((ex * 2 + rh) * 320 + (cg * 16 + ch) * 4 + g) * 16, t[0], t[1], epoch);
  }
}

extern "C" void kernel_launch(void* const* d_in, const int* in_sizes, int n_in, void* d_out, int out_size,
                              void* d_ws, size_t ws_size, hipStream_t stream) {
  Params p;
  p.x = (const unsigned short*)d_in[0];  p.k = (const unsigned short*)d_in[1];
  p.v = (const unsigned short*)d_in[2];  p.wq = (const unsigned short*)d_in[3];
  p.wo = (const unsigned short*)d_in[4]; p.wg = (const unsigned short*)d_in[6];
  p.wu = (const unsigned short*)d_in[7]; p.wd = (const unsigned short*)d_in[8];
  p.c = (const unsigned short*)d_in[9];  p.wm = (const unsigned short*)d_in[10];
  p.out = (unsigned short*)d_out;        p.ws = (char*)d_ws;
  fused_block<<<256, 768, 0, stream>>>(p);
}
```
